```python
import torch

def _softmax(x, axis=-1):
    return torch.softmax(x, dim=axis)
def _gelu(x):
    return torch.nn.functional.gelu(x, approximate="tanh")
def _silu(x):
    return torch.nn.functional.silu(x)
def _sigmoid(x):
    return torch.sigmoid(x)
def _layernorm(x, axis=-1, eps=1e-5):
    m = x.mean(dim=axis, keepdim=True)
    v = x.var(dim=axis, keepdim=True, unbiased=False)
    return (x - m) / torch.sqrt(v + eps)
def _scatter_add(tgt, idx, src, axis=0):
    return tgt.clone().index_add_(axis, idx, src)
def _segment_sum(src, seg, n):
    out = torch.zeros((n,) + src.shape[1:], dtype=src.dtype)
    return out.index_add_(0, seg, src)
def _pad(x, pw):
    flat = tuple(v for pair in reversed(pw) for v in pair)
    return torch.nn.functional.pad(x, flat)

kernel_name = 'standalone__dense_activation_silu__prompt'

def setup_inputs():

    _w = (lambda a: (lambda t: t.to(torch.bfloat16) if t.is_floating_point() else t)(torch.as_tensor(a)))
    B, S, D, F = 1, 2048, 576, 4608
    return {
        "x": _w((lambda *s: torch.randn(*s, dtype=torch.bfloat16) * 0.1)(B, S, D)),
        "w1": _w((lambda *s: torch.randn(*s, dtype=torch.bfloat16) * 3.2)(D, F) * (D ** -0.5)),
        "w2": _w((lambda *s: torch.randn(*s, dtype=torch.bfloat16) * 3.2)(F, D) * (F ** -0.5)),
    }

def reference(x, w1, w2):
    h = torch.einsum("bsd,df->bsf", x, w1)
    h = _silu(h)
    out = torch.einsum("bsf,fd->bsd", h, w2)
    return out
```

```cpp
#include <hip/hip_runtime.h>
#include <hip/hip_bf16.h>

__global__ __launch_bounds__(256, 1)
void mlp_fused(const void* px, const void* pw1, const void* pw2, void* pout, void* pws) {
  const unsigned w = __builtin_amdgcn_readfirstlane(threadIdx.x >> 6);
  const unsigned bid = blockIdx.x;
  asm volatile(
    ".Lstart:\n"
    "s_load_dwordx2 s[20:21], %0, 0\n"
    "s_load_dwordx2 s[24:25], %0, 8\n"
    "s_load_dwordx2 s[28:29], %0, 16\n"
    "s_load_dwordx2 s[36:37], %0, 24\n"
    "s_load_dwordx2 s[32:33], %0, 32\n"
    "s_mov_b32 s44, %2\n"
    "v_mbcnt_lo_u32_b32 v244, -1, 0\n"
    "v_mbcnt_hi_u32_b32 v244, -1, v244\n"
    "s_getpc_b64 s[40:41]\n"
    ".Lpc:\n"
    "s_mov_b32 s42, .Lend-.Lpc\n"
    "s_mov_b32 s43, 0x20000\n"
    "s_lshl_b32 s49, s44, 13\n"
    "v_lshlrev_b32 v242, 7, v244\n"
    "buffer_load_dword v243, v242, s[40:43], s49 offen\n"
    "s_mov_b32 s22, 0x240000\n"
    "s_mov_b32 s23, 0x20000\n"
    "s_mov_b32 s26, 0x510000\n"
    "s_mov_b32 s27, 0x20000\n"
    "s_mov_b32 s30, 0x510000\n"
    "s_mov_b32 s31, 0x20000\n"
    "s_mov_b32 s38, 0x240000\n"
    "s_mov_b32 s39, 0x20000\n"
    "s_mov_b32 s34, 0x4010000\n"
    "s_mov_b32 s35, 0x20000\n"
    "s_and_b32 s49, %1, 1\n"
    "s_lshl_b32 s49, s49, 4\n"
    "s_lshr_b32 s45, %1, 4\n"
    "s_add_u32 s45, s45, s49\n"
    "s_and_b32 s49, %1, 6\n"
    "s_bfe_u32 s46, %1, 0x10003\n"
    "s_add_u32 s46, s46, s49\n"
    "s_mov_b32 s9, %1\n"
    "s_lshl_b32 s48, s44, 11\n"
    "s_lshl_b32 s49, s45, 2\n"
    "s_add_u32 s49, s49, s44\n"
    "s_mul_i32 s69, s49, 0x4800\n"
    "s_and_b32 s49, s45, 15\n"
    "s_add_u32 s16, s49, s46\n"
    "s_sub_u32 s49, s16, 9\n"
    "s_cmp_ge_u32 s16, 9\n"
    "s_cselect_b32 s16, s49, s16\n"
    "s_sub_u32 s49, s16, 9\n"
    "s_cmp_ge_u32 s16, 9\n"
    "s_cselect_b32 s16, s49, s16\n"
    "v_lshrrev_b32 v242, 3, v244\n"
    "v_lshrrev_b32 v208, 4, v244\n"
    "v_xor_b32 v209, v244, v208\n"
    "v_and_b32 v209, 7, v209\n"
    "v_lshlrev_b32 v209, 4, v209\n"
    "s_movk_i32 s50, 0x480\n"
    "v_mad_u32_u24 v224, v242, s50, v209\n"
    "v_xor_b32 v225, 64, v224\n"
    "v_add_u32 v225, 0x2000, v225\n"
    "v_lshlrev_b32 v210, 2, v208\n"
    "v_xor_b32 v210, v244, v210\n"
    "v_and_b32 v210, 7, v210\n"
    "v_lshlrev_b32 v210, 4, v210\n"
    "v_mad_u32_u24 v241, v242, s50, v210\n"
    "s_movk_i32 s50, 0x2400\n"
    "v_mad_u32_u24 v226, v242, s50, v210\n"
    "s_lshl_b32 s49, s44, 8\n"
    "v_add_u32 v226, s49, v226\n"
    "v_add_u32 v227, 0x11c00, v226\n"
    "s_mul_i32 s62, s44, 0x23f00\n"
    "s_add_u32 s62, s62, 0x400\n"
    "s_mul_i32 s63, s46, 0x480\n"
    "s_add_u32 s50, s16, 0\n"
    "s_sub_u32 s49, s50, 9\n"
    "s_cmp_ge_u32 s50, 9\n"
    "s_cselect_b32 s50, s49, s50\n"
    "s_mul_i32 s53, s50, 0x90000\n"
    "s_add_u32 s53, s53, s63\n"
    "s_add_u32 s50, s16, 1\n"
    "s_sub_u32 s49, s50, 9\n"
    "s_cmp_ge_u32 s50, 9\n"
    "s_cselect_b32 s50, s49, s50\n"
    "s_mul_i32 s54, s50, 0x90000\n"
    "s_add_u32 s54, s54, s63\n"
    "s_add_u32 s50, s16, 2\n"
    "s_sub_u32 s49, s50, 9\n"
    "s_cmp_ge_u32 s50, 9\n"
    "s_cselect_b32 s50, s49, s50\n"
    "s_mul_i32 s55, s50, 0x90000\n"
    "s_add_u32 s55, s55, s63\n"
    "s_add_u32 s50, s16, 3\n"
    "s_sub_u32 s49, s50, 9\n"
    "s_cmp_ge_u32 s50, 9\n"
    "s_cselect_b32 s50, s49, s50\n"
    "s_mul_i32 s56, s50, 0x90000\n"
    "s_add_u32 s56, s56, s63\n"
    "s_add_u32 s50, s16, 4\n"
    "s_sub_u32 s49, s50, 9\n"
    "s_cmp_ge_u32 s50, 9\n"
    "s_cselect_b32 s50, s49, s50\n"
    "s_mul_i32 s57, s50, 0x90000\n"
    "s_add_u32 s57, s57, s63\n"
    "s_add_u32 s50, s16, 5\n"
    "s_sub_u32 s49, s50, 9\n"
    "s_cmp_ge_u32 s50, 9\n"
    "s_cselect_b32 s50, s49, s50\n"
    "s_mul_i32 s58, s50, 0x90000\n"
    "s_add_u32 s58, s58, s63\n"
    "s_add_u32 s50, s16, 6\n"
    "s_sub_u32 s49, s50, 9\n"
    "s_cmp_ge_u32 s50, 9\n"
    "s_cselect_b32 s50, s49, s50\n"
    "s_mul_i32 s59, s50, 0x90000\n"
    "s_add_u32 s59, s59, s63\n"
    "s_add_u32 s50, s16, 7\n"
    "s_sub_u32 s49, s50, 9\n"
    "s_cmp_ge_u32 s50, 9\n"
    "s_cselect_b32 s50, s49, s50\n"
    "s_mul_i32 s60, s50, 0x90000\n"
    "s_add_u32 s60, s60, s63\n"
    "s_add_u32 s50, s16, 8\n"
    "s_sub_u32 s49, s50, 9\n"
    "s_cmp_ge_u32 s50, 9\n"
    "s_cselect_b32 s50, s49, s50\n"
    "s_mul_i32 s61, s50, 0x90000\n"
    "s_add_u32 s61, s61, s63\n"
    "s_waitcnt lgkmcnt(0)\n"
    "s_add_u32 m0, s48, 0x16000\n"
    "s_add_u32 s50, s16, 0\n"
    "s_sub_u32 s49, s50, 9\n"
    "s_cmp_ge_u32 s50, 9\n"
    "s_cselect_b32 s50, s49, s50\n"
    "s_lshl_b32 s49, s50, 7\n"
    "s_add_u32 s49, s49, s69\n"
    "buffer_load_dwordx4 v224, s[20:23], s49 offen lds\n"
    "buffer_load_dwordx4 v225, s[20:23], s49 offen offset:1024 lds\n"
    "s_add_u32 m0, s48, 0x18000\n"
    "s_add_u32 s50, s16, 1\n"
    "s_sub_u32 s49, s50, 9\n"
    "s_cmp_ge_u32 s50, 9\n"
    "s_cselect_b32 s50, s49, s50\n"
    "s_lshl_b32 s49, s50, 7\n"
    "s_add_u32 s49, s49, s69\n"
    "buffer_load_dwordx4 v224, s[20:23], s49 offen lds\n"
    "buffer_load_dwordx4 v225, s[20:23], s49 offen offset:1024 lds\n"
    "s_add_u32 m0, s48, 0\n"
    "s_nop 0\n"
    "buffer_load_dwordx4 v226, s[24:27], s53 offen lds\n"
    "buffer_load_dwordx4 v227, s[24:27], s53 offen offset:1024 lds\n"
    "s_add_u32 m0, s48, 0x2000\n"
    "s_add_u32 s49, s53, 0x24000\n"
    "buffer_load_dwordx4 v226, s[24:27], s49 offen lds\n"
    "buffer_load_dwordx4 v227, s[24:27], s49 offen offset:1024 lds\n"
    "s_add_u32 m0, s48, 0x4000\n"
    "s_add_u32 s49, s53, 0x48000\n"
    "buffer_load_dwordx4 v226, s[24:27], s49 offen lds\n"
    "buffer_load_dwordx4 v227, s[24:27], s49 offen offset:1024 lds\n"
    "s_add_u32 m0, s48, 0x6000\n"
    "s_add_u32 s49, s53, 0x6c000\n"
    "buffer_load_dwordx4 v226, s[24:27], s49 offen lds\n"
    "buffer_load_dwordx4 v227, s[24:27], s49 offen offset:1024 lds\n"
    "s_add_u32 m0, s48, 0x1a000\n"
    "s_add_u32 s50, s16, 2\n"
    "s_sub_u32 s49, s50, 9\n"
    "s_cmp_ge_u32 s50, 9\n"
    "s_cselect_b32 s50, s49, s50\n"
    "s_lshl_b32 s49, s50, 7\n"
    "s_add_u32 s49, s49, s69\n"
    "buffer_load_dwordx4 v224, s[20:23], s49 offen lds\n"
    "buffer_load_dwordx4 v225, s[20:23], s49 offen offset:1024 lds\n"
    "s_add_u32 m0, s48, 0x8000\n"
    "s_add_u32 s49, s53, s62\n"
    "buffer_load_dwordx4 v226, s[24:27], s49 offen lds\n"
    "buffer_load_dwordx4 v227, s[24:27], s49 offen offset:1024 lds\n"
    "s_lshl_b32 s49, s45, 7\n"
    "s_buffer_load_dword s52, s[32:35], s49\n"
    "s_mul_i32 s63, s46, 0xa2000\n"
    "s_and_b32 s47, s44, 1\n"
    "s_lshr_b32 s49, s44, 1\n"
    "s_mul_i32 s64, s49, 0x24000\n"
    "s_mul_i32 s65, s49, 0x9000\n"
    "s_mul_i32 s50, s47, 0x4800\n"
    "s_add_u32 s50, s50, s63\n"
    "s_add_u32 s64, s64, s50\n"
    "s_add_u32 s65, s65, s50\n"
    "s_mov_b32 s51, 0xbfb8aa3b\n"
    "v_lshrrev_b32 v208, 5, v244\n"
    "v_bfe_u32 v209, v244, 1, 3\n"
    "v_xor_b32 v209, v208, v209\n"
    "v_lshlrev_b32 v209, 4, v209\n"
    "v_and_b32 v210, 31, v244\n"
    "v_lshl_add_u32 v209, v210, 7, v209\n"
    "s_lshl_b32 s50, s47, 12\n"
    "s_add_u32 s50, s50, 0x16000\n"
    "v_add_u32 v96, s50, v209\n"
    "v_xor_b32 v97, 32, v96\n"
    "v_xor_b32 v98, 64, v96\n"
    "v_xor_b32 v99, 0x60, v96\n"
    "v_xor_b32 v100, 0x1000, v96\n"
    "v_xor_b32 v101, 0x1020, v96\n"
    "v_xor_b32 v102, 0x1040, v96\n"
    "v_xor_b32 v103, 0x1060, v96\n"
    "v_bfe_u32 v209, v244, 2, 2\n"
    "v_and_b32 v210, 3, v244\n"
    "v_lshlrev_b32 v210, 3, v210\n"
    "v_bfe_u32 v211, v244, 4, 1\n"
    "v_lshl_add_u32 v210, v211, 5, v210\n"
    "v_bfe_u32 v211, v244, 3, 1\n"
    "v_lshl_add_u32 v210, v211, 6, v210\n"
    "v_lshl_add_u32 v210, v209, 7, v210\n"
    "s_lshl_b32 s50, s49, 6\n"
    "v_lshl_add_u32 v230, v208, 10, v210\n"
    "v_add_u32 v228, s48, v230\n"
    "v_xor_b32 v232, 64, v228\n"
    "v_add_u32 v229, 0x10000, v228\n"
    "v_add_u32 v231, 0x20000, v228\n"
    "v_add_u32 v246, 0x10000, v232\n"
    "v_add_u32 v247, 0x20000, v232\n"
    "v_xor_b32 v230, s50, v230\n"
    "v_lshlrev_b32 v248, 9, v208\n"
    "v_sub_u32 v233, v230, v248\n"
    "v_lshlrev_b32 v239, 4, v244\n"
    "v_lshlrev_b32 v240, 2, v244\n"
    "v_and_b32 v240, 60, v240\n"
    "s_lshl_b32 s50, s47, 13\n"
    "s_add_u32 s50, s50, 0x24000\n"
    "v_add_u32 v234, s50, v239\n"
    "v_xor_b32 v235, 0x2000, v234\n"
    "v_add_u32 v236, s48, v234\n"
    "v_add_u32 v237, s48, v235\n"
    "s_lshl_b32 s50, s49, 11\n"
    "v_add_u32 v238, s50, v234\n"
    "v_and_b32 v209, 31, v244\n"
    "v_lshlrev_b32 v210, 3, v208\n"
    "s_movk_i32 s50, 0x480\n"
    "v_mad_u32_u24 v245, v209, s50, v210\n"
    "s_lshl_b32 s13, s45, 1\n"
    "s_add_u32 s13, s13, s47\n"
    "s_mul_i32 s13, s13, 0x9000\n"
    "s_lshl_b32 s50, s49, 6\n"
    "s_add_u32 s13, s13, s50\n"
    "s_lshl_b32 s50, s46, 7\n"
    "s_add_u32 s13, s13, s50\n"
    "s_bfe_u32 s50, s46, 0x10001\n"
    "s_lshl_b32 s15, s45, 1\n"
    "s_add_u32 s15, s15, s50\n"
    "s_mul_i32 s15, s15, 0x9000\n"
    "s_lshr_b32 s50, s46, 2\n"
    "s_lshl_b32 s50, s50, 6\n"
    "s_add_u32 s15, s15, s50\n"
    "s_and_b32 s50, s46, 1\n"
    "s_lshl_b32 s50, s50, 5\n"
    "s_add_u32 s15, s15, s50\n"
    "s_add_u32 s15, s15, 0x400\n"
    "s_mul_i32 s12, s45, 0x80\n"
    "s_add_u32 s12, s12, 0x2000\n"
    "s_add_u32 s50, s46, 1\n"
    "s_and_b32 s50, s50, 7\n"
    "s_lshl_b32 s18, 15, s50\n"
    "s_lshr_b32 s50, s18, 8\n"
    "s_or_b32 s18, s18, s50\n"
    "s_and_b32 s18, s18, 0xff\n"
    "s_lshl_b32 s18, s18, 8\n"
    "s_mov_b32 s19, 0\n"
    "v_bfe_u32 v212, v239, 7, 2\n"
    "v_bfe_u32 v213, v239, 5, 1\n"
    "v_lshl_add_u32 v212, v212, 1, v213\n"
    "v_bfe_u32 v213, v239, 6, 1\n"
    "v_lshrrev_b32 v214, 9, v239\n"
    "v_xor_b32 v213, v213, v214\n"
    "v_lshl_add_u32 v212, v213, 3, v212\n"
    "v_and_b32 v213, 16, v239\n"
    "v_mov_b32 v214, 0x3f80\n"
    "v_lshlrev_b32 v214, v213, v214\n"
    "v_cmp_eq_u32 s[66:67], 0, v212\n"
    "s_nop 1\n"
    "v_cndmask_b32 v216, 0, v214, s[66:67]\n"
    "v_cmp_eq_u32 s[66:67], 1, v212\n"
    "s_nop 1\n"
    "v_cndmask_b32 v217, 0, v214, s[66:67]\n"
    "v_cmp_eq_u32 s[66:67], 2, v212\n"
    "s_nop 1\n"
    "v_cndmask_b32 v218, 0, v214, s[66:67]\n"
    "v_cmp_eq_u32 s[66:67], 3, v212\n"
    "s_nop 1\n"
    "v_cndmask_b32 v219, 0, v214, s[66:67]\n"
    "v_cmp_eq_u32 s[66:67], 4, v212\n"
    "s_nop 1\n"
    "v_cndmask_b32 v220, 0, v214, s[66:67]\n"
    "v_cmp_eq_u32 s[66:67], 5, v212\n"
    "s_nop 1\n"
    "v_cndmask_b32 v221, 0, v214, s[66:67]\n"
    "v_cmp_eq_u32 s[66:67], 6, v212\n"
    "s_nop 1\n"
    "v_cndmask_b32 v222, 0, v214, s[66:67]\n"
    "v_cmp_eq_u32 s[66:67], 7, v212\n"
    "s_nop 1\n"
    "v_cndmask_b32 v223, 0, v214, s[66:67]\n"
    "s_waitcnt lgkmcnt(0)\n"
    "s_add_u32 s52, s52, 1\n"
    "s_and_b32 s68, s52, 3\n"
    "s_mul_i32 s68, s68, 0x1000000\n"
    "s_add_u32 s68, s68, 0x10000\n"
    "s_lshl_b32 s49, s45, 3\n"
    "s_add_u32 s49, s49, s46\n"
    "s_mul_i32 s11, s49, 0xe000\n"
    "s_add_u32 s11, s11, s68\n"
    "s_add_u32 s11, s11, s48\n"
    "s_lshl_b32 s14, s49, 13\n"
    "s_add_u32 s14, s14, s68\n"
    "s_add_u32 s14, s14, 0xe00000\n"
    "s_waitcnt vmcnt(14)\n"
    "s_barrier\n"
    "ds_read_b128 a[0:3], v96 offset:0\n"
    "ds_read_b128 a[4:7], v100 offset:0\n"
    "ds_read_b128 a[8:11], v97 offset:0\n"
    "ds_read_b128 a[12:15], v101 offset:0\n"
    "ds_read_b128 a[16:19], v98 offset:0\n"
    "ds_read_b128 a[20:23], v102 offset:0\n"
    "ds_read_b128 a[24:27], v99 offset:0\n"
    "ds_read_b128 a[28:31], v103 offset:0\n"
    "s_waitcnt vmcnt(10)\n"
    "ds_read_b64_tr_b16 v[176:177], v228 offset:0\n"
    "ds_read_b64_tr_b16 v[178:179], v228 offset:512\n"
    "ds_read_b64_tr_b16 v[180:181], v232 offset:0\n"
    "ds_read_b64_tr_b16 v[182:183], v232 offset:512\n"
    "s_waitcnt vmcnt(8)\n"
    "ds_read_b64_tr_b16 v[184:185], v228 offset:8192\n"
    "ds_read_b64_tr_b16 v[186:187], v228 offset:8704\n"
    "ds_read_b64_tr_b16 v[188:189], v232 offset:8192\n"
    "ds_read_b64_tr_b16 v[190:191], v232 offset:8704\n"
    "s_waitcnt vmcnt(6) lgkmcnt(13)\n"
    "ds_read_b64_tr_b16 v[192:193], v228 offset:16384\n"
    "ds_read_b64_tr_b16 v[194:195], v228 offset:16896\n"
    "s_waitcnt lgkmcnt(8)\n"
    "v_mfma_f32_32x32x16_bf16 v[32:47], v[176:179], a[0:3], 0\n"
    "s_add_u32 m0, s48, 0xa000\n"
    "s_nop 0\n"
    "buffer_load_dwordx4 v226, s[24:27], s54 offen lds\n"
    "v_mfma_f32_32x32x16_bf16 v[48:63], v[176:179], a[4:7], 0\n"
    "buffer_load_dwordx4 v227, s[24:27], s54 offen offset:1024 lds\n"
    "ds_read_b64_tr_b16 v[196:197], v232 offset:16384\n"
    "ds_read_b64_tr_b16 v[198:199], v232 offset:16896\n"
    "s_waitcnt lgkmcnt(8)\n"
    "v_mfma_f32_32x32x16_bf16 v[64:79], v[180:183], a[0:3], 0\n"
    "s_add_u32 m0, s48, 0xc000\n"
    "s_add_u32 s49, s54, 0x24000\n"
    "buffer_load_dwordx4 v226, s[24:27], s49 offen lds\n"
    "v_mfma_f32_32x32x16_bf16 v[80:95], v[180:183], a[4:7], 0\n"
    "buffer_load_dwordx4 v227, s[24:27], s49 offen offset:1024 lds\n"
    "s_waitcnt vmcnt(8) lgkmcnt(4)\n"
    "ds_read_b64_tr_b16 v[200:201], v228 offset:24576\n"
    "ds_read_b64_tr_b16 v[202:203], v228 offset:25088\n"
    "v_mfma_f32_32x32x16_bf16 v[32:47], v[184:187], a[8:11], v[32:47]\n"
    "s_add_u32 m0, s48, 0xe000\n"
    "s_add_u32 s49, s54, 0x48000\n"
    "buffer_load_dwordx4 v226, s[24:27], s49 offen lds\n"
    "v_mfma_f32_32x32x16_bf16 v[48:63], v[184:187], a[12:15], v[48:63]\n"
    "buffer_load_dwordx4 v227, s[24:27], s49 offen offset:1024 lds\n"
    "ds_read_b64_tr_b16 v[204:205], v232 offset:24576\n"
    "ds_read_b64_tr_b16 v[206:207], v232 offset:25088\n"
    "v_mfma_f32_32x32x16_bf16 v[64:79], v[188:191], a[8:11], v[64:79]\n"
    "s_add_u32 m0, s48, 0x10000\n"
    "s_add_u32 s49, s54, 0x6c000\n"
    "buffer_load_dwordx4 v226, s[24:27], s49 offen lds\n"
    "v_mfma_f32_32x32x16_bf16 v[80:95], v[188:191], a[12:15], v[80:95]\n"
    "buffer_load_dwordx4 v227, s[24:27], s49 offen offset:1024 lds\n"
    "s_waitcnt vmcnt(8)\n"
    "s_barrier\n"
    "ds_read_b64_tr_b16 v[176:177], v230 offset:32768\n"
    "ds_read_b64_tr_b16 v[178:179], v230 offset:33280\n"
    "s_waitcnt lgkmcnt(6)\n"
    "v_mfma_f32_32x32x16_bf16 v[32:47], v[192:195], a[16:19], v[32:47]\n"
    "s_add_u32 m0, s48, 0x1c000\n"
    "s_add_u32 s50, s16, 3\n"
    "s_sub_u32 s49, s50, 9\n"
    "s_cmp_ge_u32 s50, 9\n"
    "s_cselect_b32 s50, s49, s50\n"
    "s_lshl_b32 s49, s50, 7\n"
    "s_add_u32 s49, s49, s69\n"
    "buffer_load_dwordx4 v224, s[20:23], s49 offen lds\n"
    "ds_read_b128 a[32:35], v96 offset:8192\n"
    "ds_read_b128 a[36:39], v100 offset:8192\n"
    "ds_read_b64_tr_b16 v[180:181], v230 offset:34816\n"
    "ds_read_b64_tr_b16 v[182:183], v230 offset:35328\n"
    "v_mfma_f32_32x32x16_bf16 v[48:63], v[192:195], a[20:23], v[48:63]\n"
    "buffer_load_dwordx4 v225, s[20:23], s49 offen offset:1024 lds\n"
    "ds_read_b128 a[40:43], v97 offset:8192\n"
    "ds_read_b128 a[44:47], v101 offset:8192\n"
    "ds_read_b64_tr_b16 v[184:185], v230 offset:36864\n"
    "ds_read_b64_tr_b16 v[186:187], v230 offset:37376\n"
    "v_mfma_f32_32x32x16_bf16 v[64:79], v[196:199], a[16:19], v[64:79]\n"
    "s_add_u32 m0, s48, 0x12000\n"
    "s_add_u32 s49, s54, s62\n"
    "buffer_load_dwordx4 v226, s[24:27], s49 offen lds\n"
    "ds_read_b128 a[48:51], v98 offset:8192\n"
    "ds_read_b128 a[52:55], v102 offset:8192\n"
    "ds_read_b64_tr_b16 v[188:189], v230 offset:38912\n"
    "ds_read_b64_tr_b16 v[190:191], v230 offset:39424\n"
    "v_mfma_f32_32x32x16_bf16 v[80:95], v[196:199], a[20:23], v[80:95]\n"
    "buffer_load_dwordx4 v227, s[24:27], s49 offen offset:1024 lds\n"
    "ds_read_b128 a[56:59], v99 offset:8192\n"
    "ds_read_b128 a[60:63], v103 offset:8192\n"
    "s_waitcnt vmcnt(10) lgkmcnt(13)\n"
    "ds_read_b64_tr_b16 v[192:193], v228 offset:40960\n"
    "ds_read_b64_tr_b16 v[194:195], v228 offset:41472\n"
    "v_mfma_f32_32x32x16_bf16 v[32:47], v[200:203], a[24:27], v[32:47]\n"
    "s_add_u32 m0, s48, 0x14000\n"
    "s_nop 0\n"
    "buffer_load_dwordx4 v226, s[24:27], s55 offen lds\n"
    "ds_read_b128 a[64:67], v96 offset:16384\n"
    "ds_read_b128 a[68:71], v100 offset:16384\n"
    "v_mfma_f32_32x32x16_bf16 v[48:63], v[200:203], a[28:31], v[48:63]\n"
    "buffer_load_dwordx4 v227, s[24:27], s55 offen offset:1024 lds\n"
    "ds_read_b128 a[72:75], v97 offset:16384\n"
    "ds_read_b128 a[76:79], v101 offset:16384\n"
    "ds_read_b64_tr_b16 v[196:197], v232 offset:40960\n"
    "ds_read_b64_tr_b16 v[198:199], v232 offset:41472\n"
    "v_mfma_f32_32x32x16_bf16 v[64:79], v[204:207], a[24:27], v[64:79]\n"
    "s_add_u32 m0, s48, 0x16000\n"
    "s_add_u32 s49, s55, 0x24000\n"
    "buffer_load_dwordx4 v226, s[24:27], s49 offen lds\n"
    "ds_read_b128 a[80:83], v98 offset:16384\n"
    "ds_read_b128 a[84:87], v102 offset:16384\n"
    "v_mfma_f32_32x32x16_bf16 v[80:95], v[204:207], a[28:31], v[80:95]\n"
    "buffer_load_dwordx4 v227, s[24:27], s49 offen offset:1024 lds\n"
    "ds_read_b128 a[88:91], v99 offset:16384\n"
    "ds_read_b128 a[92:95], v103 offset:16384\n"
    "s_waitcnt vmcnt(12) lgkmcnt(13)\n"
    "ds_read_b64_tr_b16 v[200:201], v228 offset:49152\n"
    "ds_read_b64_tr_b16 v[202:203], v228 offset:49664\n"
    "v_mfma_f32_32x32x16_bf16 v[160:175], v[176:179], a[0:3], 0\n"
    "s_add_u32 m0, s48, 0\n"
    "s_add_u32 s49, s55, 0x48000\n"
    "buffer_load_dwordx4 v226, s[24:27], s49 offen lds\n"
    "v_mfma_f32_32x32x16_bf16 v[160:175], v[180:183], a[8:11], v[160:175]\n"
    "buffer_load_dwordx4 v227, s[24:27], s49 offen offset:1024 lds\n"
    "ds_read_b64_tr_b16 v[204:205], v232 offset:49152\n"
    "ds_read_b64_tr_b16 v[206:207], v232 offset:49664\n"
    "v_mfma_f32_32x32x16_bf16 v[160:175], v[184:187], a[16:19], v[160:175]\n"
    "s_add_u32 m0, s48, 0x2000\n"
    "s_add_u32 s49, s55, 0x6c000\n"
    "buffer_load_dwordx4 v226, s[24:27], s49 offen lds\n"
    "v_mfma_f32_32x32x16_bf16 v[160:175], v[188:191], a[24:27], v[160:175]\n"
    "buffer_load_dwordx4 v227, s[24:27], s49 offen offset:1024 lds\n"
    "s_waitcnt vmcnt(14) lgkmcnt(8)\n"
    "ds_read_b64_tr_b16 v[176:177], v228 offset:57344\n"
    "ds_read_b64_tr_b16 v[178:179], v228 offset:57856\n"
    "v_mfma_f32_32x32x16_bf16 v[32:47], v[192:195], a[32:35], v[32:47]\n"
    "s_add_u32 m0, s48, 0x1e000\n"
    "s_add_u32 s50, s16, 4\n"
    "s_sub_u32 s49, s50, 9\n"
    "s_cmp_ge_u32 s50, 9\n"
    "s_cselect_b32 s50, s49, s50\n"
    "s_lshl_b32 s49, s50, 7\n"
    "s_add_u32 s49, s49, s69\n"
    "buffer_load_dwordx4 v224, s[20:23], s49 offen lds\n"
    "v_mfma_f32_32x32x16_bf16 v[48:63], v[192:195], a[36:39], v[48:63]\n"
    "buffer_load_dwordx4 v225, s[20:23], s49 offen offset:1024 lds\n"
    "ds_read_b64_tr_b16 v[180:181], v232 offset:57344\n"
    "ds_read_b64_tr_b16 v[182:183], v232 offset:57856\n"
    "v_mfma_f32_32x32x16_bf16 v[64:79], v[196:199], a[32:35], v[64:79]\n"
    "s_add_u32 m0, s48, 0x4000\n"
    "s_add_u32 s49, s55, s62\n"
    "buffer_load_dwordx4 v226, s[24:27], s49 offen lds\n"
    "v_mfma_f32_32x32x16_bf16 v[80:95], v[196:199], a[36:39], v[80:95]\n"
    "buffer_load_dwordx4 v227, s[24:27], s49 offen offset:1024 lds\n"
    "s_waitcnt vmcnt(16) lgkmcnt(4)\n"
    "ds_read_b64_tr_b16 v[184:185], v229 offset:0\n"
    "ds_read_b64_tr_b16 v[186:187], v229 offset:512\n"
    "v_mfma_f32_32x32x16_bf16 v[32:47], v[200:203], a[40:43], v[32:47]\n"
    "s_add_u32 m0, s48, 0x6000\n"
    "s_nop 0\n"
    "buffer_load_dwordx4 v226, s[24:27], s56 offen lds\n"
    "v_mfma_f32_32x32x16_bf16 v[48:63], v[200:203], a[44:47], v[48:63]\n"
    "ds_read_b64_tr_b16 v[188:189], v246 offset:0\n"
    "ds_read_b64_tr_b16 v[190:191], v246 offset:512\n"
    "v_mfma_f32_32x32x16_bf16 v[64:79], v[204:207], a[40:43], v[64:79]\n"
    "buffer_load_dwordx4 v227, s[24:27], s56 offen offset:1024 lds\n"
    "v_mfma_f32_32x32x16_bf16 v[80:95], v[204:207], a[44:47], v[80:95]\n"
    "s_add_u32 m0, s48, 0xa000\n"
    "s_add_u32 s49, s56, 0x24000\n"
    "buffer_load_dwordx4 v226, s[24:27], s49 offen lds\n"
    "s_waitcnt vmcnt(15)\n"
    "s_barrier\n"
    "v_add_u32 v230, 0x12000, v230\n"
    "ds_read_b64_tr_b16 v[192:193], v230 offset:0\n"
    "ds_read_b64_tr_b16 v[194:195], v230 offset:512\n"
    "s_waitcnt lgkmcnt(6)\n"
    "v_mfma_f32_32x32x16_bf16 v[32:47], v[176:179], a[48:51], v[32:47]\n"
    "buffer_load_dwordx4 v227, s[24:27], s49 offen offset:1024 lds\n"
    "ds_read_b128 a[96:99], v96 offset:24576\n"
    "ds_read_b128 a[100:103], v100 offset:24576\n"
    "ds_read_b64_tr_b16 v[196:197], v230 offset:2048\n"
    "ds_read_b64_tr_b16 v[198:199], v230 offset:2560\n"
    "v_mfma_f32_32x32x16_bf16 v[48:63], v[176:179], a[52:55], v[48:63]\n"
    "s_add_u32 m0, s48, 0xc000\n"
    "s_add_u32 s49, s56, 0x48000\n"
    "buffer_load_dwordx4 v226, s[24:27], s49 offen lds\n"
    "ds_read_b128 a[104:107], v97 offset:24576\n"
    "ds_read_b128 a[108:111], v101 offset:24576\n"
    "ds_read_b64_tr_b16 v[200:201], v230 offset:4096\n"
    "ds_read_b64_tr_b16 v[202:203], v230 offset:4608\n"
    "v_mfma_f32_32x32x16_bf16 v[64:79], v[180:183], a[48:51], v[64:79]\n"
    "buffer_load_dwordx4 v227, s[24:27], s49 offen offset:1024 lds\n"
    "ds_read_b128 a[112:115], v98 offset:24576\n"
    "ds_read_b128 a[116:119], v102 offset:24576\n"
    "ds_read_b64_tr_b16 v[204:205], v230 offset:6144\n"
    "ds_read_b64_tr_b16 v[206:207], v230 offset:6656\n"
    "v_mfma_f32_32x32x16_bf16 v[80:95], v[180:183], a[52:55], v[80:95]\n"
    "s_add_u32 m0, s48, 0xe000\n"
    "s_add_u32 s49, s56, 0x6c000\n"
    "buffer_load_dwordx4 v226, s[24:27], s49 offen lds\n"
    "ds_read_b128 a[120:123], v99 offset:24576\n"
    "ds_read_b128 a[124:127], v103 offset:24576\n"
    "s_waitcnt vmcnt(17) lgkmcnt(13)\n"
    "ds_read_b64_tr_b16 v[176:177], v229 offset:16384\n"
    "ds_read_b64_tr_b16 v[178:179], v229 offset:16896\n"
    "v_mfma_f32_32x32x16_bf16 v[32:47], v[184:187], a[56:59], v[32:47]\n"
    "buffer_load_dwordx4 v227, s[24:27], s49 offen offset:1024 lds\n"
    "v_mfma_f32_32x32x16_bf16 v[48:63], v[184:187], a[60:63], v[48:63]\n"
    "s_add_u32 m0, s48, 0x20000\n"
    "s_add_u32 s50, s16, 5\n"
    "s_sub_u32 s49, s50, 9\n"
    "s_cmp_ge_u32 s50, 9\n"
    "s_cselect_b32 s50, s49, s50\n"
    "s_lshl_b32 s49, s50, 7\n"
    "s_add_u32 s49, s49, s69\n"
    "buffer_load_dwordx4 v224, s[20:23], s49 offen lds\n"
    "ds_read_b64_tr_b16 v[180:181], v246 offset:16384\n"
    "ds_read_b64_tr_b16 v[182:183], v246 offset:16896\n"
    "v_mfma_f32_32x32x16_bf16 v[64:79], v[188:191], a[56:59], v[64:79]\n"
    "buffer_load_dwordx4 v225, s[20:23], s49 offen offset:1024 lds\n"
    "v_mfma_f32_32x32x16_bf16 v[80:95], v[188:191], a[60:63], v[80:95]\n"
    "s_add_u32 m0, s48, 0x10000\n"
    "s_add_u32 s49, s56, s62\n"
    "buffer_load_dwordx4 v226, s[24:27], s49 offen lds\n"
    "s_waitcnt vmcnt(19) lgkmcnt(13)\n"
    "ds_read_b64_tr_b16 v[184:185], v229 offset:24576\n"
    "ds_read_b64_tr_b16 v[186:187], v229 offset:25088\n"
    "v_mfma_f32_32x32x16_bf16 v[160:175], v[192:195], a[32:35], v[160:175]\n"
    "buffer_load_dwordx4 v227, s[24:27], s49 offen offset:1024 lds\n"
    "v_mfma_f32_32x32x16_bf16 v[160:175], v[196:199], a[40:43], v[160:175]\n"
    "s_add_u32 m0, s48, 0x18000\n"
    "s_nop 0\n"
    "buffer_load_dwordx4 v226, s[24:27], s57 offen lds\n"
    "ds_read_b64_tr_b16 v[188:189], v246 offset:24576\n"
    "ds_read_b64_tr_b16 v[190:191], v246 offset:25088\n"
    "s_waitcnt lgkmcnt(10)\n"
    "v_mfma_f32_32x32x16_bf16 v[160:175], v[200:203], a[48:51], v[160:175]\n"
    "buffer_load_dwordx4 v227, s[24:27], s57 offen offset:1024 lds\n"
    "v_mfma_f32_32x32x16_bf16 v[160:175], v[204:207], a[56:59], v[160:175]\n"
    "s_add_u32 m0, s48, 0x1a000\n"
    "s_add_u32 s49, s57, 0x24000\n"
    "buffer_load_dwordx4 v226, s[24:27], s49 offen lds\n"
    "s_waitcnt vmcnt(21) lgkmcnt(4)\n"
    "ds_read_b64_tr_b16 v[192:193], v228 offset:0\n"
    "ds_read_b64_tr_b16 v[194:195], v228 offset:512\n"
    "v_mfma_f32_32x32x16_bf16 v[32:47], v[176:179], a[64:67], v[32:47]\n"
    "v_mfma_f32_32x32x16_bf16 v[48:63], v[176:179], a[68:71], v[48:63]\n"
    "buffer_load_dwordx4 v227, s[24:27], s49 offen offset:1024 lds\n"
    "ds_read_b64_tr_b16 v[196:197], v232 offset:0\n"
    "ds_read_b64_tr_b16 v[198:199], v232 offset:512\n"
    "v_mfma_f32_32x32x16_bf16 v[64:79], v[180:183], a[64:67], v[64:79]\n"
    "s_add_u32 m0, s48, 0x8000\n"
    "s_add_u32 s49, s57, 0x48000\n"
    "buffer_load_dwordx4 v226, s[24:27], s49 offen lds\n"
    "v_mfma_f32_32x32x16_bf16 v[80:95], v[180:183], a[68:71], v[80:95]\n"
    "s_waitcnt vmcnt(21) lgkmcnt(4)\n"
    "ds_read_b64_tr_b16 v[200:201], v228 offset:8192\n"
    "ds_read_b64_tr_b16 v[202:203], v228 offset:8704\n"
    "v_mfma_f32_32x32x16_bf16 v[32:47], v[184:187], a[72:75], v[32:47]\n"
    "buffer_load_dwordx4 v227, s[24:27], s49 offen offset:1024 lds\n"
    "v_mfma_f32_32x32x16_bf16 v[48:63], v[184:187], a[76:79], v[48:63]\n"
    "ds_read_b64_tr_b16 v[204:205], v232 offset:8192\n"
    "ds_read_b64_tr_b16 v[206:207], v232 offset:8704\n"
    "v_mfma_f32_32x32x16_bf16 v[64:79], v[188:191], a[72:75], v[64:79]\n"
    "s_add_u32 m0, s48, 0x14000\n"
    "s_add_u32 s49, s57, 0x6c000\n"
    "buffer_load_dwordx4 v226, s[24:27], s49 offen lds\n"
    "v_mfma_f32_32x32x16_bf16 v[80:95], v[188:191], a[76:79], v[80:95]\n"
    "s_waitcnt vmcnt(19)\n"
    "s_barrier\n"
    "v_add_u32 v230, 0xffff2000, v230\n"
    "ds_read_b64_tr_b16 v[176:177], v230 offset:0\n"
    "ds_read_b64_tr_b16 v[178:179], v230 offset:512\n"
    "s_waitcnt lgkmcnt(6)\n"
    "v_mfma_f32_32x32x16_bf16 v[32:47], v[192:195], a[80:83], v[32:47]\n"
    "buffer_load_dwordx4 v227, s[24:27], s49 offen offset:1024 lds\n"
    "ds_read_b128 a[128:131], v96 offset:32768\n"
    "ds_read_b128 a[132:135], v100 offset:32768\n"
    "ds_read_b64_tr_b16 v[180:181], v230 offset:2048\n"
    "ds_read_b64_tr_b16 v[182:183], v230 offset:2560\n"
    "v_mfma_f32_32x32x16_bf16 v[48:63], v[192:195], a[84:87], v[48:63]\n"
    "s_add_u32 m0, s48, 0x22000\n"
    "s_add_u32 s50, s16, 6\n"
    "s_sub_u32 s49, s50, 9\n"
    "s_cmp_ge_u32 s50, 9\n"
    "s_cselect_b32 s50, s49, s50\n"
    "s_lshl_b32 s49, s50, 7\n"
    "s_add_u32 s49, s49, s69\n"
    "buffer_load_dwordx4 v224, s[20:23], s49 offen lds\n"
    "ds_read_b128 a[136:139], v97 offset:32768\n"
    "ds_read_b128 a[140:143], v101 offset:32768\n"
    "ds_read_b64_tr_b16 v[184:185], v230 offset:4096\n"
    "ds_read_b64_tr_b16 v[186:187], v230 offset:4608\n"
    "v_mfma_f32_32x32x16_bf16 v[64:79], v[196:199], a[80:83], v[64:79]\n"
    "buffer_load_dwordx4 v225, s[20:23], s49 offen offset:1024 lds\n"
    "ds_read_b128 a[144:147], v98 offset:32768\n"
    "ds_read_b128 a[148:151], v102 offset:32768\n"
    "ds_read_b64_tr_b16 v[188:189], v230 offset:6144\n"
    "ds_read_b64_tr_b16 v[190:191], v230 offset:6656\n"
    "v_mfma_f32_32x32x16_bf16 v[80:95], v[196:199], a[84:87], v[80:95]\n"
    "s_add_u32 m0, s48, 0x16000\n"
    "s_add_u32 s49, s57, s62\n"
    "buffer_load_dwordx4 v226, s[24:27], s49 offen lds\n"
    "ds_read_b128 a[152:155], v99 offset:32768\n"
    "ds_read_b128 a[156:159], v103 offset:32768\n"
    "s_waitcnt vmcnt(21) lgkmcnt(13)\n"
    "ds_read_b64_tr_b16 v[192:193], v228 offset:24576\n"
    "ds_read_b64_tr_b16 v[194:195], v228 offset:25088\n"
    "v_mfma_f32_32x32x16_bf16 v[32:47], v[200:203], a[88:91], v[32:47]\n"
    "buffer_load_dwordx4 v227, s[24:27], s49 offen offset:1024 lds\n"
    "v_mfma_f32_32x32x16_bf16 v[48:63], v[200:203], a[92:95], v[48:63]\n"
    "s_add_u32 m0, s48, 0x1c000\n"
    "s_nop 0\n"
    "buffer_load_dwordx4 v226, s[24:27], s58 offen lds\n"
    "ds_read_b64_tr_b16 v[196:197], v232 offset:24576\n"
    "ds_read_b64_tr_b16 v[198:199], v232 offset:25088\n"
    "v_mfma_f32_32x32x16_bf16 v[64:79], v[204:207], a[88:91], v[64:79]\n"
    "buffer_load_dwordx4 v227, s[24:27], s58 offen offset:1024 lds\n"
    "v_mfma_f32_32x32x16_bf16 v[80:95], v[204:207], a[92:95], v[80:95]\n"
    "s_add_u32 m0, s48, 0\n"
    "s_add_u32 s49, s58, 0x24000\n"
    "buffer_load_dwordx4 v226, s[24:27], s49 offen lds\n"
    "s_waitcnt vmcnt(23) lgkmcnt(13)\n"
    "ds_read_b64_tr_b16 v[200:201], v228 offset:40960\n"
    "ds_read_b64_tr_b16 v[202:203], v228 offset:41472\n"
    "v_mfma_f32_32x32x16_bf16 v[160:175], v[176:179], a[64:67], v[160:175]\n"
    "buffer_load_dwordx4 v227, s[24:27], s49 offen offset:1024 lds\n"
    "v_mfma_f32_32x32x16_bf16 v[160:175], v[180:183], a[72:75], v[160:175]\n"
    "s_add_u32 m0, s48, 0x2000\n"
    "s_add_u32 s49, s58, 0x48000\n"
    "buffer_load_dwordx4 v226, s[24:27], s49 offen lds\n"
    "ds_read_b64_tr_b16 v[204:205], v232 offset:40960\n"
    "ds_read_b64_tr_b16 v[206:207], v232 offset:41472\n"
    "s_waitcnt lgkmcnt(10)\n"
    "v_mfma_f32_32x32x16_bf16 v[160:175], v[184:187], a[80:83], v[160:175]\n"
    "v_mfma_f32_32x32x16_bf16 v[160:175], v[188:191], a[88:91], v[160:175]\n"
    "buffer_load_dwordx4 v227, s[24:27], s49 offen offset:1024 lds\n"
    "s_waitcnt vmcnt(24) lgkmcnt(4)\n"
    "ds_read_b64_tr_b16 v[176:177], v228 offset:49152\n"
    "ds_read_b64_tr_b16 v[178:179], v228 offset:49664\n"
    "v_mfma_f32_32x32x16_bf16 v[32:47], v[192:195], a[96:99], v[32:47]\n"
    "v_mfma_f32_32x32x16_bf16 v[48:63], v[192:195], a[100:103], v[48:63]\n"
    "s_add_u32 m0, s48, 0x12000\n"
    "s_add_u32 s49, s58, 0x6c000\n"
    "buffer_load_dwordx4 v226, s[24:27], s49 offen lds\n"
    "ds_read_b64_tr_b16 v[180:181], v232 offset:49152\n"
    "ds_read_b64_tr_b16 v[182:183], v232 offset:49664\n"
    "v_mfma_f32_32x32x16_bf16 v[64:79], v[196:199], a[96:99], v[64:79]\n"
    "v_mfma_f32_32x32x16_bf16 v[80:95], v[196:199], a[100:103], v[80:95]\n"
    "buffer_load_dwordx4 v227, s[24:27], s49 offen offset:1024 lds\n"
    "s_waitcnt vmcnt(24) lgkmcnt(4)\n"
    "ds_read_b64_tr_b16 v[184:185], v228 offset:57344\n"
    "ds_read_b64_tr_b16 v[186:187], v228 offset:57856\n"
    "v_mfma_f32_32x32x16_bf16 v[32:47], v[200:203], a[104:107], v[32:47]\n"
    "v_mfma_f32_32x32x16_bf16 v[48:63], v[200:203], a[108:111], v[48:63]\n"
    "s_add_u32 m0, s48, 0x24000\n"
    "s_add_u32 s50, s16, 7\n"
    "s_sub_u32 s49, s50, 9\n"
    "s_cmp_ge_u32 s50, 9\n"
    "s_cselect_b32 s50, s49, s50\n"
    "s_lshl_b32 s49, s50, 7\n"
    "s_add_u32 s49, s49, s69\n"
    "buffer_load_dwordx4 v224, s[20:23], s49 offen lds\n"
    "ds_read_b64_tr_b16 v[188:189], v232 offset:57344\n"
    "ds_read_b64_tr_b16 v[190:191], v232 offset:57856\n"
    "v_mfma_f32_32x32x16_bf16 v[64:79], v[204:207], a[104:107], v[64:79]\n"
    "buffer_load_dwordx4 v225, s[20:23], s49 offen offset:1024 lds\n"
    "v_mfma_f32_32x32x16_bf16 v[80:95], v[204:207], a[108:111], v[80:95]\n"
    "s_add_u32 m0, s48, 0x6000\n"
    "s_add_u32 s49, s58, s62\n"
    "buffer_load_dwordx4 v226, s[24:27], s49 offen lds\n"
    "s_waitcnt vmcnt(23)\n"
    "s_barrier\n"
    "ds_read_b64_tr_b16 v[192:193], v230 offset:49152\n"
    "ds_read_b64_tr_b16 v[194:195], v230 offset:49664\n"
    "s_waitcnt lgkmcnt(6)\n"
    "v_mfma_f32_32x32x16_bf16 v[32:47], v[176:179], a[112:115], v[32:47]\n"
    "buffer_load_dwordx4 v227, s[24:27], s49 offen offset:1024 lds\n"
    "ds_read_b128 a[160:163], v96 offset:40960\n"
    "ds_read_b128 a[164:167], v100 offset:40960\n"
    "ds_read_b64_tr_b16 v[196:197], v230 offset:51200\n"
    "ds_read_b64_tr_b16 v[198:199], v230 offset:51712\n"
    "v_mfma_f32_32x32x16_bf16 v[48:63], v[176:179], a[116:119], v[48:63]\n"
    "s_add_u32 m0, s48, 0xa000\n"
    "s_nop 0\n"
    "buffer_load_dwordx4 v226, s[24:27], s59 offen lds\n"
    "ds_read_b128 a[168:171], v97 offset:40960\n"
    "ds_read_b128 a[172:175], v101 offset:40960\n"
    "ds_read_b64_tr_b16 v[200:201], v230 offset:53248\n"
    "ds_read_b64_tr_b16 v[202:203], v230 offset:53760\n"
    "v_mfma_f32_32x32x16_bf16 v[64:79], v[180:183], a[112:115], v[64:79]\n"
    "buffer_load_dwordx4 v227, s[24:27], s59 offen offset:1024 lds\n"
    "ds_read_b128 a[176:179], v98 offset:40960\n"
    "ds_read_b128 a[180:183], v102 offset:40960\n"
    "ds_read_b64_tr_b16 v[204:205], v230 offset:55296\n"
    "ds_read_b64_tr_b16 v[206:207], v230 offset:55808\n"
    "v_mfma_f32_32x32x16_bf16 v[80:95], v[180:183], a[116:119], v[80:95]\n"
    "s_add_u32 m0, s48, 0xc000\n"
    "s_add_u32 s49, s59, 0x24000\n"
    "buffer_load_dwordx4 v226, s[24:27], s49 offen lds\n"
    "ds_read_b128 a[184:187], v99 offset:40960\n"
    "ds_read_b128 a[188:191], v103 offset:40960\n"
    "s_waitcnt vmcnt(25) lgkmcnt(13)\n"
    "ds_read_b64_tr_b16 v[176:177], v229 offset:32768\n"
    "ds_read_b64_tr_b16 v[178:179], v229 offset:33280\n"
    "v_mfma_f32_32x32x16_bf16 v[32:47], v[184:187], a[120:123], v[32:47]\n"
    "buffer_load_dwordx4 v227, s[24:27], s49 offen offset:1024 lds\n"
    "v_mfma_f32_32x32x16_bf16 v[48:63], v[184:187], a[124:127], v[48:63]\n"
    "s_add_u32 m0, s48, 0x1e000\n"
    "s_add_u32 s49, s59, 0x48000\n"
    "buffer_load_dwordx4 v226, s[24:27], s49 offen lds\n"
    "ds_read_b64_tr_b16 v[180:181], v246 offset:32768\n"
    "ds_read_b64_tr_b16 v[182:183], v246 offset:33280\n"
    "v_mfma_f32_32x32x16_bf16 v[64:79], v[188:191], a[120:123], v[64:79]\n"
    "buffer_load_dwordx4 v227, s[24:27], s49 offen offset:1024 lds\n"
    "v_mfma_f32_32x32x16_bf16 v[80:95], v[188:191], a[124:127], v[80:95]\n"
    "s_add_u32 m0, s48, 0x4000\n"
    "s_add_u32 s49, s59, 0x6c000\n"
    "buffer_load_dwordx4 v226, s[24:27], s49 offen lds\n"
    "s_waitcnt vmcnt(27) lgkmcnt(13)\n"
    "ds_read_b64_tr_b16 v[184:185], v229 offset:40960\n"
    "ds_read_b64_tr_b16 v[186:187], v229 offset:41472\n"
    "v_mfma_f32_32x32x16_bf16 v[160:175], v[192:195], a[96:99], v[160:175]\n"
    "v_mfma_f32_32x32x16_bf16 v[160:175], v[196:199], a[104:107], v[160:175]\n"
    "buffer_load_dwordx4 v227, s[24:27], s49 offen offset:1024 lds\n"
    "ds_read_b64_tr_b16 v[188:189], v246 offset:40960\n"
    "ds_read_b64_tr_b16 v[190:191], v246 offset:41472\n"
    "s_waitcnt lgkmcnt(10)\n"
    "v_mfma_f32_32x32x16_bf16 v[160:175], v[200:203], a[112:115], v[160:175]\n"
    "v_mfma_f32_32x32x16_bf16 v[160:175], v[204:207], a[120:123], v[160:175]\n"
    "s_add_u32 m0, s48, 0x26000\n"
    "s_add_u32 s50, s16, 8\n"
    "s_sub_u32 s49, s50, 9\n"
    "s_cmp_ge_u32 s50, 9\n"
    "s_cselect_b32 s50, s49, s50\n"
    "s_lshl_b32 s49, s50, 7\n"
    "s_add_u32 s49, s49, s69\n"
    "buffer_load_dwordx4 v224, s[20:23], s49 offen lds\n"
    "s_waitcnt vmcnt(27) lgkmcnt(4)\n"
    "ds_read_b64_tr_b16 v[192:193], v228 offset:32768\n"
    "ds_read_b64_tr_b16 v[194:195], v228 offset:33280\n"
    "v_mfma_f32_32x32x16_bf16 v[32:47], v[176:179], a[128:131], v[32:47]\n"
    "v_mfma_f32_32x32x16_bf16 v[48:63], v[176:179], a[132:135], v[48:63]\n"
    "buffer_load_dwordx4 v225, s[20:23], s49 offen offset:1024 lds\n"
    "ds_read_b64_tr_b16 v[196:197], v232 offset:32768\n"
    "ds_read_b64_tr_b16 v[198:199], v232 offset:33280\n"
    "v_mfma_f32_32x32x16_bf16 v[64:79], v[180:183], a[128:131], v[64:79]\n"
    "s_add_u32 m0, s48, 0xe000\n"
    "s_add_u32 s49, s59, s62\n"
    "buffer_load_dwordx4 v226, s[24:27], s49 offen lds\n"
    "v_mfma_f32_32x32x16_bf16 v[80:95], v[180:183], a[132:135], v[80:95]\n"
    "s_waitcnt vmcnt(27) lgkmcnt(4)\n"
    "ds_read_b64_tr_b16 v[200:201], v229 offset:16384\n"
    "ds_read_b64_tr_b16 v[202:203], v229 offset:16896\n"
    "v_mfma_f32_32x32x16_bf16 v[32:47], v[184:187], a[136:139], v[32:47]\n"
    "buffer_load_dwordx4 v227, s[24:27], s49 offen offset:1024 lds\n"
    "v_mfma_f32_32x32x16_bf16 v[48:63], v[184:187], a[140:143], v[48:63]\n"
    "ds_read_b64_tr_b16 v[204:205], v246 offset:16384\n"
    "ds_read_b64_tr_b16 v[206:207], v246 offset:16896\n"
    "v_mfma_f32_32x32x16_bf16 v[64:79], v[188:191], a[136:139], v[64:79]\n"
    "s_add_u32 m0, s48, 0x18000\n"
    "s_nop 0\n"
    "buffer_load_dwordx4 v226, s[24:27], s60 offen lds\n"
    "v_mfma_f32_32x32x16_bf16 v[80:95], v[188:191], a[140:143], v[80:95]\n"
    "s_waitcnt vmcnt(25)\n"
    "s_barrier\n"
    "v_add_u32 v230, 0x12000, v230\n"
    "ds_read_b64_tr_b16 v[176:177], v230 offset:0\n"
    "ds_read_b64_tr_b16 v[178:179], v230 offset:512\n"
    "s_waitcnt lgkmcnt(6)\n"
    "v_mfma_f32_32x32x16_bf16 v[32:47], v[192:195], a[144:147], v[32:47]\n"
    "buffer_load_dwordx4 v227, s[24:27], s60 offen offset:1024 lds\n"
    "ds_read_b128 a[192:195], v96 offset:49152\n"
    "ds_read_b128 a[196:199], v100 offset:49152\n"
    "ds_read_b64_tr_b16 v[180:181], v230 offset:2048\n"
    "ds_read_b64_tr_b16 v[182:183], v230 offset:2560\n"
    "v_mfma_f32_32x32x16_bf16 v[48:63], v[192:195], a[148:151], v[48:63]\n"
    "s_add_u32 m0, s48, 0x1a000\n"
    "s_add_u32 s49, s60, 0x24000\n"
    "buffer_load_dwordx4 v226, s[24:27], s49 offen lds\n"
    "ds_read_b128 a[200:203], v97 offset:49152\n"
    "ds_read_b128 a[204:207], v101 offset:49152\n"
    "ds_read_b64_tr_b16 v[184:185], v230 offset:4096\n"
    "ds_read_b64_tr_b16 v[186:187], v230 offset:4608\n"
    "v_mfma_f32_32x32x16_bf16 v[64:79], v[196:199], a[144:147], v[64:79]\n"
    "buffer_load_dwordx4 v227, s[24:27], s49 offen offset:1024 lds\n"
    "ds_read_b128 a[208:211], v98 offset:49152\n"
    "ds_read_b128 a[212:215], v102 offset:49152\n"
    "ds_read_b64_tr_b16 v[188:189], v230 offset:6144\n"
    "ds_read_b64_tr_b16 v[190:191], v230 offset:6656\n"
    "v_mfma_f32_32x32x16_bf16 v[80:95], v[196:199], a[148:151], v[80:95]\n"
    "s_add_u32 m0, s48, 0x20000\n"
    "s_add_u32 s49, s60, 0x48000\n"
    "buffer_load_dwordx4 v226, s[24:27], s49 offen lds\n"
    "ds_read_b128 a[216:219], v99 offset:49152\n"
    "ds_read_b128 a[220:223], v103 offset:49152\n"
    "s_waitcnt vmcnt(27) lgkmcnt(13)\n"
    "ds_read_b64_tr_b16 v[192:193], v229 offset:49152\n"
    "ds_read_b64_tr_b16 v[194:195], v229 offset:49664\n"
    "v_mfma_f32_32x32x16_bf16 v[32:47], v[200:203], a[152:155], v[32:47]\n"
    "buffer_load_dwordx4 v227, s[24:27], s49 offen offset:1024 lds\n"
    "v_mfma_f32_32x32x16_bf16 v[48:63], v[200:203], a[156:159], v[48:63]\n"
    "s_add_u32 m0, s48, 0x8000\n"
    "s_add_u32 s49, s60, 0x6c000\n"
    "buffer_load_dwordx4 v226, s[24:27], s49 offen lds\n"
    "ds_read_b64_tr_b16 v[196:197], v246 offset:49152\n"
    "ds_read_b64_tr_b16 v[198:199], v246 offset:49664\n"
    "v_mfma_f32_32x32x16_bf16 v[64:79], v[204:207], a[152:155], v[64:79]\n"
    "buffer_load_dwordx4 v227, s[24:27], s49 offen offset:1024 lds\n"
    "v_mfma_f32_32x32x16_bf16 v[80:95], v[204:207], a[156:159], v[80:95]\n"
    "s_add_u32 m0, s48, 0x10000\n"
    "s_add_u32 s49, s60, s62\n"
    "buffer_load_dwordx4 v226, s[24:27], s49 offen lds\n"
    "s_waitcnt vmcnt(29) lgkmcnt(13)\n"
    "ds_read_b64_tr_b16 v[200:201], v228 offset:0\n"
    "ds_read_b64_tr_b16 v[202:203], v228 offset:512\n"
    "v_mfma_f32_32x32x16_bf16 v[160:175], v[176:179], a[128:131], v[160:175]\n"
    "v_mfma_f32_32x32x16_bf16 v[160:175], v[180:183], a[136:139], v[160:175]\n"
    "buffer_load_dwordx4 v227, s[24:27], s49 offen offset:1024 lds\n"
    "ds_read_b64_tr_b16 v[204:205], v232 offset:0\n"
    "ds_read_b64_tr_b16 v[206:207], v232 offset:512\n"
    "s_waitcnt lgkmcnt(10)\n"
    "v_mfma_f32_32x32x16_bf16 v[160:175], v[184:187], a[144:147], v[160:175]\n"
    "v_mfma_f32_32x32x16_bf16 v[160:175], v[188:191], a[152:155], v[160:175]\n"
    "s_add_u32 m0, s48, 0x14000\n"
    "s_nop 0\n"
    "buffer_load_dwordx4 v226, s[24:27], s61 offen lds\n"
    "s_waitcnt vmcnt(29) lgkmcnt(4)\n"
    "ds_read_b64_tr_b16 v[176:177], v228 offset:8192\n"
    "ds_read_b64_tr_b16 v[178:179], v228 offset:8704\n"
    "v_mfma_f32_32x32x16_bf16 v[32:47], v[192:195], a[160:163], v[32:47]\n"
    "v_mfma_f32_32x32x16_bf16 v[48:63], v[192:195], a[164:167], v[48:63]\n"
    "buffer_load_dwordx4 v227, s[24:27], s61 offen offset:1024 lds\n"
    "ds_read_b64_tr_b16 v[180:181], v232 offset:8192\n"
    "ds_read_b64_tr_b16 v[182:183], v232 offset:8704\n"
    "v_mfma_f32_32x32x16_bf16 v[64:79], v[196:199], a[160:163], v[64:79]\n"
    "v_mfma_f32_32x32x16_bf16 v[80:95], v[196:199], a[164:167], v[80:95]\n"
    "s_add_u32 m0, s48, 0x1c000\n"
    "s_add_u32 s49, s61, 0x24000\n"
    "buffer_load_dwordx4 v226, s[24:27], s49 offen lds\n"
    "s_waitcnt vmcnt(29) lgkmcnt(4)\n"
    "ds_read_b64_tr_b16 v[184:185], v229 offset:8192\n"
    "ds_read_b64_tr_b16 v[186:187], v229 offset:8704\n"
    "v_mfma_f32_32x32x16_bf16 v[32:47], v[200:203], a[168:171], v[32:47]\n"
    "v_mfma_f32_32x32x16_bf16 v[48:63], v[200:203], a[172:175], v[48:63]\n"
    "buffer_load_dwordx4 v227, s[24:27], s49 offen offset:1024 lds\n"
    "ds_read_b64_tr_b16 v[188:189], v246 offset:8192\n"
    "ds_read_b64_tr_b16 v[190:191], v246 offset:8704\n"
    "v_mfma_f32_32x32x16_bf16 v[64:79], v[204:207], a[168:171], v[64:79]\n"
    "v_mfma_f32_32x32x16_bf16 v[80:95], v[204:207], a[172:175], v[80:95]\n"
    "s_add_u32 m0, s48, 0\n"
    "s_add_u32 s49, s61, 0x48000\n"
    "buffer_load_dwordx4 v226, s[24:27], s49 offen lds\n"
    "s_waitcnt vmcnt(27)\n"
    "s_barrier\n"
    "v_add_u32 v230, 0xffff0000, v230\n"
    "ds_read_b64_tr_b16 v[192:193], v230 offset:0\n"
    "ds_read_b64_tr_b16 v[194:195], v230 offset:512\n"
    "s_waitcnt lgkmcnt(6)\n"
    "v_mfma_f32_32x32x16_bf16 v[32:47], v[176:179], a[176:179], v[32:47]\n"
    "buffer_load_dwordx4 v227, s[24:27], s49 offen offset:1024 lds\n"
    "ds_read_b128 a[224:227], v96 offset:57344\n"
    "ds_read_b128 a[228:231], v100 offset:57344\n"
    "ds_read_b64_tr_b16 v[196:197], v230 offset:2048\n"
    "ds_read_b64_tr_b16 v[198:199], v230 offset:2560\n"
    "v_mfma_f32_32x32x16_bf16 v[48:63], v[176:179], a[180:183], v[48:63]\n"
    "s_add_u32 m0, s48, 0x16000\n"
    "s_add_u32 s49, s61, 0x6c000\n"
    "buffer_load_dwordx4 v226, s[24:27], s49 offen lds\n"
    "ds_read_b128 a[232:235], v97 offset:57344\n"
    "ds_read_b128 a[236:239], v101 offset:57344\n"
    "ds_read_b64_tr_b16 v[200:201], v230 offset:4096\n"
    "ds_read_b64_tr_b16 v[202:203], v230 offset:4608\n"
    "v_mfma_f32_32x32x16_bf16 v[64:79], v[180:183], a[176:179], v[64:79]\n"
    "buffer_load_dwordx4 v227, s[24:27], s49 offen offset:1024 lds\n"
    "ds_read_b128 a[240:243], v98 offset:57344\n"
    "ds_read_b128 a[244:247], v102 offset:57344\n"
    "ds_read_b64_tr_b16 v[204:205], v230 offset:6144\n"
    "ds_read_b64_tr_b16 v[206:207], v230 offset:6656\n"
    "v_mfma_f32_32x32x16_bf16 v[80:95], v[180:183], a[180:183], v[80:95]\n"
    "s_add_u32 m0, s48, 0x22000\n"
    "s_add_u32 s49, s61, s62\n"
    "buffer_load_dwordx4 v226, s[24:27], s49 offen lds\n"
    "ds_read_b128 a[248:251], v99 offset:57344\n"
    "ds_read_b128 a[252:255], v103 offset:57344\n"
    "s_waitcnt vmcnt(29) lgkmcnt(13)\n"
    "ds_read_b64_tr_b16 v[176:177], v228 offset:40960\n"
    "ds_read_b64_tr_b16 v[178:179], v228 offset:41472\n"
    "v_mfma_f32_32x32x16_bf16 v[32:47], v[184:187], a[184:187], v[32:47]\n"
    "v_mfma_f32_32x32x16_bf16 v[48:63], v[184:187], a[188:191], v[48:63]\n"
    "buffer_load_dwordx4 v227, s[24:27], s49 offen offset:1024 lds\n"
    "ds_read_b64_tr_b16 v[180:181], v232 offset:40960\n"
    "ds_read_b64_tr_b16 v[182:183], v232 offset:41472\n"
    "v_mfma_f32_32x32x16_bf16 v[64:79], v[188:191], a[184:187], v[64:79]\n"
    "s_add_u32 m0, s48, 0x2000\n"
    "s_add_u32 s49, s53, 0x80\n"
    "buffer_load_dwordx4 v226, s[24:27], s49 offen lds\n"
    "v_mfma_f32_32x32x16_bf16 v[80:95], v[188:191], a[188:191], v[80:95]\n"
    "s_waitcnt vmcnt(29) lgkmcnt(13)\n"
    "ds_read_b64_tr_b16 v[184:185], v228 offset:49152\n"
    "ds_read_b64_tr_b16 v[186:187], v228 offset:49664\n"
    "v_mfma_f32_32x32x16_bf16 v[160:175], v[192:195], a[160:163], v[160:175]\n"
    "buffer_load_dwordx4 v227, s[24:27], s49 offen offset:1024 lds\n"
    "v_mfma_f32_32x32x16_bf16 v[160:175], v[196:199], a[168:171], v[160:175]\n"
    "ds_read_b64_tr_b16 v[188:189], v232 offset:49152\n"
    "ds_read_b64_tr_b16 v[190:191], v232 offset:49664\n"
    "s_waitcnt lgkmcnt(10)\n"
    "v_mfma_f32_32x32x16_bf16 v[160:175], v[200:203], a[176:179], v[160:175]\n"
    "s_add_u32 m0, s48, 0x12000\n"
    "s_add_u32 s49, s53, 0x24080\n"
    "buffer_load_dwordx4 v226, s[24:27], s49 offen lds\n"
    "v_mfma_f32_32x32x16_bf16 v[160:175], v[204:207], a[184:187], v[160:175]\n"
    "s_waitcnt vmcnt(29) lgkmcnt(4)\n"
    "ds_read_b64_tr_b16 v[192:193], v229 offset:57344\n"
    "ds_read_b64_tr_b16 v[194:195], v229 offset:57856\n"
    "v_mfma_f32_32x32x16_bf16 v[32:47], v[176:179], a[192:195], v[32:47]\n"
    "buffer_load_dwordx4 v227, s[24:27], s49 offen offset:1024 lds\n"
    "v_mfma_f32_32x32x16_bf16 v[48:63], v[176:179], a[196:199], v[48:63]\n"
    "ds_read_b64_tr_b16 v[196:197], v246 offset:57344\n"
    "ds_read_b64_tr_b16 v[198:199], v246 offset:57856\n"
    "v_mfma_f32_32x32x16_bf16 v[64:79], v[180:183], a[192:195], v[64:79]\n"
    "s_add_u32 m0, s48, 0xa000\n"
    "s_add_u32 s49, s53, 0x48080\n"
    "buffer_load_dwordx4 v226, s[24:27], s49 offen lds\n"
    "v_mfma_f32_32x32x16_bf16 v[80:95], v[180:183], a[196:199], v[80:95]\n"
    "s_waitcnt vmcnt(29) lgkmcnt(4)\n"
    "ds_read_b64_tr_b16 v[200:201], v228 offset:16384\n"
    "ds_read_b64_tr_b16 v[202:203], v228 offset:16896\n"
    "v_mfma_f32_32x32x16_bf16 v[32:47], v[184:187], a[200:203], v[32:47]\n"
    "buffer_load_dwordx4 v227, s[24:27], s49 offen offset:1024 lds\n"
    "v_mfma_f32_32x32x16_bf16 v[48:63], v[184:187], a[204:207], v[48:63]\n"
    "ds_read_b64_tr_b16 v[204:205], v232 offset:16384\n"
    "ds_read_b64_tr_b16 v[206:207], v232 offset:16896\n"
    "v_mfma_f32_32x32x16_bf16 v[64:79], v[188:191], a[200:203], v[64:79]\n"
    "s_add_u32 m0, s48, 0xc000\n"
    "s_add_u32 s49, s53, 0x6c080\n"
    "buffer_load_dwordx4 v226, s[24:27], s49 offen lds\n"
    "v_mfma_f32_32x32x16_bf16 v[80:95], v[188:191], a[204:207], v[80:95]\n"
    "s_waitcnt vmcnt(27)\n"
    "s_barrier\n"
    "ds_read_b64_tr_b16 v[176:177], v230 offset:32768\n"
    "ds_read_b64_tr_b16 v[178:179], v230 offset:33280\n"
    "s_waitcnt lgkmcnt(6)\n"
    "v_mfma_f32_32x32x16_bf16 v[32:47], v[192:195], a[208:211], v[32:47]\n"
    "buffer_load_dwordx4 v227, s[24:27], s49 offen offset:1024 lds\n"
    "v_add_u32 v96, 0x2000, v96\n"
    "ds_read_b128 v[0:3], v96 offset:57344\n"
    "v_add_u32 v100, 0x2000, v100\n"
    "ds_read_b128 v[4:7], v100 offset:57344\n"
    "ds_read_b64_tr_b16 v[180:181], v230 offset:34816\n"
    "ds_read_b64_tr_b16 v[182:183], v230 offset:35328\n"
    "v_mfma_f32_32x32x16_bf16 v[48:63], v[192:195], a[212:215], v[48:63]\n"
    "v_add_u32 v97, 0x2000, v97\n"
    "ds_read_b128 v[8:11], v97 offset:57344\n"
    "v_add_u32 v101, 0x2000, v101\n"
    "ds_read_b128 v[12:15], v101 offset:57344\n"
    "ds_read_b64_tr_b16 v[184:185], v230 offset:36864\n"
    "ds_read_b64_tr_b16 v[186:187], v230 offset:37376\n"
    "v_mfma_f32_32x32x16_bf16 v[64:79], v[196:199], a[208:211], v[64:79]\n"
    "s_add_u32 m0, s48, 0x1e000\n"
    "s_add_u32 s49, s54, 0x80\n"
    "buffer_load_dwordx4 v226, s[24:27], s49 offen lds\n"
    "v_add_u32 v98, 0x2000, v98\n"
    "ds_read_b128 v[16:19], v98 offset:57344\n"
    "v_add_u32 v102, 0x2000, v102\n"
    "ds_read_b128 v[20:23], v102 offset:57344\n"
    "ds_read_b64_tr_b16 v[188:189], v230 offset:38912\n"
    "ds_read_b64_tr_b16 v[190:191], v230 offset:39424\n"
    "v_mfma_f32_32x32x16_bf16 v[80:95], v[196:199], a[212:215], v[80:95]\n"
    "v_add_u32 v99, 0x2000, v99\n"
    "ds_read_b128 v[24:27], v99 offset:57344\n"
    "v_add_u32 v103, 0x2000, v103\n"
    "ds_read_b128 v[28:31], v103 offset:57344\n"
    "s_waitcnt vmcnt(27) lgkmcnt(13)\n"
    "ds_read_b64_tr_b16 v[192:193], v229 offset:32768\n"
    "ds_read_b64_tr_b16 v[194:195], v229 offset:33280\n"
    "v_mfma_f32_32x32x16_bf16 v[32:47], v[200:203], a[216:219], v[32:47]\n"
    "buffer_load_dwordx4 v227, s[24:27], s49 offen offset:1024 lds\n"
    "v_mfma_f32_32x32x16_bf16 v[48:63], v[200:203], a[220:223], v[48:63]\n"
    "ds_read_b64_tr_b16 v[196:197], v246 offset:32768\n"
    "ds_read_b64_tr_b16 v[198:199], v246 offset:33280\n"
    "v_mfma_f32_32x32x16_bf16 v[64:79], v[204:207], a[216:219], v[64:79]\n"
    "s_add_u32 m0, s48, 0x4000\n"
    "s_add_u32 s49, s54, 0x24080\n"
    "buffer_load_dwordx4 v226, s[24:27], s49 offen lds\n"
    "v_mfma_f32_32x32x16_bf16 v[80:95], v[204:207], a[220:223], v[80:95]\n"
    "s_waitcnt vmcnt(27) lgkmcnt(13)\n"
    "ds_read_b64_tr_b16 v[200:201], v229 offset:40960\n"
    "ds_read_b64_tr_b16 v[202:203], v229 offset:41472\n"
    "v_mfma_f32_32x32x16_bf16 v[160:175], v[176:179], a[192:195], v[160:175]\n"
    "buffer_load_dwordx4 v227, s[24:27], s49 offen offset:1024 lds\n"
    "v_mfma_f32_32x32x16_bf16 v[160:175], v[180:183], a[200:203], v[160:175]\n"
    "ds_read_b64_tr_b16 v[204:205], v246 offset:40960\n"
    "ds_read_b64_tr_b16 v[206:207], v246 offset:41472\n"
    "s_waitcnt lgkmcnt(10)\n"
    "v_mfma_f32_32x32x16_bf16 v[160:175], v[184:187], a[208:211], v[160:175]\n"
    "s_add_u32 m0, s48, 0x6000\n"
    "s_add_u32 s49, s54, 0x48080\n"
    "buffer_load_dwordx4 v226, s[24:27], s49 offen lds\n"
    "v_mfma_f32_32x32x16_bf16 v[160:175], v[188:191], a[216:219], v[160:175]\n"
    "s_waitcnt vmcnt(27) lgkmcnt(4)\n"
    "ds_read_b64_tr_b16 v[176:177], v231 offset:0\n"
    "ds_read_b64_tr_b16 v[178:179], v231 offset:512\n"
    "v_mfma_f32_32x32x16_bf16 v[32:47], v[192:195], a[224:227], v[32:47]\n"
    "buffer_load_dwordx4 v227, s[24:27], s49 offen offset:1024 lds\n"
    "v_mfma_f32_32x32x16_bf16 v[48:63], v[192:195], a[228:231], v[48:63]\n"
    "ds_read_b64_tr_b16 v[180:181], v247 offset:0\n"
    "ds_read_b64_tr_b16 v[182:183], v247 offset:512\n"
    "v_mfma_f32_32x32x16_bf16 v[64:79], v[196:199], a[224:227], v[64:79]\n"
    "s_add_u32 m0, s48, 0x18000\n"
    "s_add_u32 s49, s54, 0x6c080\n"
    "buffer_load_dwordx4 v226, s[24:27], s49 offen lds\n"
    "v_mfma_f32_32x32x16_bf16 v[80:95], v[196:199], a[228:231], v[80:95]\n"
    "s_waitcnt vmcnt(27) lgkmcnt(4)\n"
    "ds_read_b64_tr_b16 v[184:185], v228 offset:32768\n"
    "ds_read_b64_tr_b16 v[186:187], v228 offset:33280\n"
    "v_mfma_f32_32x32x16_bf16 v[32:47], v[200:203], a[232:235], v[32:47]\n"
    "buffer_load_dwordx4 v227, s[24:27], s49 offen offset:1024 lds\n"
    "v_mfma_f32_32x32x16_bf16 v[48:63], v[200:203], a[236:239], v[48:63]\n"
    "ds_read_b64_tr_b16 v[188:189], v232 offset:32768\n"
    "ds_read_b64_tr_b16 v[190:191], v232 offset:33280\n"
    "v_mfma_f32_32x32x16_bf16 v[64:79], v[204:207], a[232:235], v[64:79]\n"
    "s_add_u32 m0, s48, 0x1a000\n"
    "s_add_u32 s49, s55, 0x80\n"
    "buffer_load_dwordx4 v226, s[24:27], s49 offen lds\n"
    "v_mfma_f32_32x32x16_bf16 v[80:95], v[204:207], a[236:239], v[80:95]\n"
    "s_waitcnt vmcnt(27)\n"
    "s_barrier\n"
    "ds_read_b64_tr_b16 v[192:193], v230 offset:40960\n"
    "ds_read_b64_tr_b16 v[194:195], v230 offset:41472\n"
    "s_waitcnt lgkmcnt(6)\n"
    "v_mfma_f32_32x32x16_bf16 v[32:47], v[176:179], a[240:243], v[32:47]\n"
    "buffer_load_dwordx4 v227, s[24:27], s49 offen offset:1024 lds\n"
    "ds_read_b64_tr_b16 v[196:197], v230 offset:43008\n"
    "ds_read_b64_tr_b16 v[198:199], v230 offset:43520\n"
    "v_mfma_f32_32x32x16_bf16 v[48:63], v[176:179], a[244:247], v[48:63]\n"
    "ds_read_b64_tr_b16 v[200:201], v230 offset:45056\n"
    "ds_read_b64_tr_b16 v[202:203], v230 offset:45568\n"
    "v_mfma_f32_32x32x16_bf16 v[64:79], v[180:183], a[240:243], v[64:79]\n"
    "s_add_u32 m0, s48, 0x20000\n"
    "s_add_u32 s49, s55, 0x24080\n"
    "buffer_load_dwordx4 v226, s[24:27], s49 offen lds\n"
    "ds_read_b64_tr_b16 v[204:205], v230 offset:47104\n"
    "ds_read_b64_tr_b16 v[206:207], v230 offset:47616\n"
    "v_mfma_f32_32x32x16_bf16 v[80:95], v[180:183], a[244:247], v[80:95]\n"
    "s_waitcnt vmcnt(27) lgkmcnt(8)\n"
    "ds_read_b64_tr_b16 v[176:177], v229 offset:16384\n"
    "ds_read_b64_tr_b16 v[178:179], v229 offset:16896\n"
    "v_mfma_f32_32x32x16_bf16 v[32:47], v[184:187], a[248:251], v[32:47]\n"
    "buffer_load_dwordx4 v227, s[24:27], s49 offen offset:1024 lds\n"
    "v_mfma_f32_32x32x16_bf16 v[48:63], v[184:187], a[252:255], v[48:63]\n"
    "ds_read_b64_tr_b16 v[180:181], v246 offset:16384\n"
    "ds_read_b64_tr_b16 v[182:183], v246 offset:16896\n"
    "v_mfma_f32_32x32x16_bf16 v[64:79], v[188:191], a[248:251], v[64:79]\n"
    "s_add_u32 m0, s48, 0x8000\n"
    "s_add_u32 s49, s55, 0x48080\n"
    "buffer_load_dwordx4 v226, s[24:27], s49 offen lds\n"
    "v_mfma_f32_32x32x16_bf16 v[80:95], v[188:191], a[252:255], v[80:95]\n"
    "s_waitcnt vmcnt(27) lgkmcnt(8)\n"
    "ds_read_b64_tr_b16 v[184:185], v229 offset:49152\n"
    "ds_read_b64_tr_b16 v[186:187], v229 offset:49664\n"
    "v_mfma_f32_32x32x16_bf16 v[160:175], v[192:195], a[224:227], v[160:175]\n"
    "buffer_load_dwordx4 v227, s[24:27], s49 offen offset:1024 lds\n"
    "v_mfma_f32_32x32x16_bf16 v[160:175], v[196:199], a[232:235], v[160:175]\n"
    "ds_read_b64_tr_b16 v[188:189], v246 offset:49152\n"
    "ds_read_b64_tr_b16 v[190:191], v246 offset:49664\n"
    "s_waitcnt lgkmcnt(8)\n"
    "v_mfma_f32_32x32x16_bf16 v[160:175], v[200:203], a[240:243], v[160:175]\n"
    "s_add_u32 m0, s48, 0xe000\n"
    "s_add_u32 s49, s55, 0x6c080\n"
    "buffer_load_dwordx4 v226, s[24:27], s49 offen lds\n"
    "v_mfma_f32_32x32x16_bf16 v[160:175], v[204:207], a[248:251], v[160:175]\n"
    "s_waitcnt vmcnt(27) lgkmcnt(4)\n"
    "ds_read_b64_tr_b16 v[192:193], v228 offset:0\n"
    "ds_read_b64_tr_b16 v[194:195], v228 offset:512\n"
    "v_mfma_f32_32x32x16_bf16 v[32:47], v[176:179], v[0:3], v[32:47]\n"
    "buffer_load_dwordx4 v227, s[24:27], s49 offen offset:1024 lds\n"
    "v_mfma_f32_32x32x16_bf16 v[48:63], v[176:179], v[4:7], v[48:63]\n"
    "ds_read_b64_tr_b16 v[196:197], v232 offset:0\n"
    "ds_read_b64_tr_b16 v[198:199], v232 offset:512\n"
    "v_mfma_f32_32x32x16_bf16 v[64:79], v[180:183], v[0:3], v[64:79]\n"
    "s_add_u32 m0, s48, 0x14000\n"
    "s_add_u32 s49, s56, 0x80\n"
    "buffer_load_dwordx4 v226, s[24:27], s49 offen lds\n"
    "v_mfma_f32_32x32x16_bf16 v[80:95], v[180:183], v[4:7], v[80:95]\n"
    "s_waitcnt vmcnt(27) lgkmcnt(4)\n"
    "ds_read_b64_tr_b16 v[200:201], v229 offset:24576\n"
    "ds_read_b64_tr_b16 v[202:203], v229 offset:25088\n"
    "v_mfma_f32_32x32x16_bf16 v[32:47], v[184:187], v[8:11], v[32:47]\n"
    "buffer_load_dwordx4 v227, s[24:27], s49 offen offset:1024 lds\n"
    "v_mfma_f32_32x32x16_bf16 v[48:63], v[184:187], v[12:15], v[48:63]\n"
    "ds_read_b64_tr_b16 v[204:205], v246 offset:24576\n"
    "ds_read_b64_tr_b16 v[206:207], v246 offset:25088\n"
    "v_mfma_f32_32x32x16_bf16 v[64:79], v[188:191], v[8:11], v[64:79]\n"
    "s_add_u32 m0, s48, 0x1c000\n"
    "s_add_u32 s49, s56, 0x24080\n"
    "buffer_load_dwordx4 v226, s[24:27], s49 offen lds\n"
    "v_mfma_f32_32x32x16_bf16 v[80:95], v[188:191], v[12:15], v[80:95]\n"
    "s_waitcnt vmcnt(27)\n"
    "s_barrier\n"
    "v_add_u32 v230, 0x12000, v230\n"
    "ds_read_b64_tr_b16 v[176:177], v230 offset:40960\n"
    "ds_read_b64_tr_b16 v[178:179], v230 offset:41472\n"
    "s_waitcnt lgkmcnt(6)\n"
    "v_mfma_f32_32x32x16_bf16 v[32:47], v[192:195], v[16:19], v[32:47]\n"
    "buffer_load_dwordx4 v227, s[24:27], s49 offen offset:1024 lds\n"
    "ds_read_b64_tr_b16 v[180:181], v230 offset:43008\n"
    "ds_read_b64_tr_b16 v[182:183], v230 offset:43520\n"
    "v_mfma_f32_32x32x16_bf16 v[48:63], v[192:195], v[20:23], v[48:63]\n"
    "ds_read_b64_tr_b16 v[184:185], v230 offset:45056\n"
    "ds_read_b64_tr_b16 v[186:187], v230 offset:45568\n"
    "v_mfma_f32_32x32x16_bf16 v[64:79], v[196:199], v[16:19], v[64:79]\n"
    "s_add_u32 m0, s48, 0\n"
    "s_add_u32 s49, s56, 0x48080\n"
    "buffer_load_dwordx4 v226, s[24:27], s49 offen lds\n"
    "ds_read_b64_tr_b16 v[188:189], v230 offset:47104\n"
    "ds_read_b64_tr_b16 v[190:191], v230 offset:47616\n"
    "v_mfma_f32_32x32x16_bf16 v[80:95], v[196:199], v[20:23], v[80:95]\n"
    "s_waitcnt vmcnt(27) lgkmcnt(8)\n"
    "ds_read_b64_tr_b16 v[192:193], v228 offset:8192\n"
    "ds_read_b64_tr_b16 v[194:195], v228 offset:8704\n"
    "v_mfma_f32_32x32x16_bf16 v[32:47], v[200:203], v[24:27], v[32:47]\n"
    "buffer_load_dwordx4 v227, s[24:27], s49 offen offset:1024 lds\n"
    "v_mfma_f32_32x32x16_bf16 v[48:63], v[200:203], v[28:31], v[48:63]\n"
    "ds_read_b64_tr_b16 v[196:197], v232 offset:8192\n"
    "ds_read_b64_tr_b16 v[198:199], v232 offset:8704\n"
    "v_mfma_f32_32x32x16_bf16 v[64:79], v[204:207], v[24:27], v[64:79]\n"
    "s_add_u32 m0, s48, 0x10000\n"
    "s_add_u32 s49, s56, 0x6c080\n"
    "buffer_load_dwordx4 v226, s[24:27], s49 offen lds\n"
    "v_mfma_f32_32x32x16_bf16 v[80:95], v[204:207], v[28:31], v[80:95]\n"
    "s_waitcnt vmcnt(27) lgkmcnt(8)\n"
    "ds_read_b64_tr_b16 v[200:201], v229 offset:8192\n"
    "ds_read_b64_tr_b16 v[202:203], v229 offset:8704\n"
    "v_mfma_f32_32x32x16_bf16 v[160:175], v[176:179], v[0:3], v[160:175]\n"
    "buffer_load_dwordx4 v227, s[24:27], s49 offen offset:1024 lds\n"
    "v_mfma_f32_32x32x16_bf16 v[160:175], v[180:183], v[8:11], v[160:175]\n"
    "ds_read_b64_tr_b16 v[204:205], v246 offset:8192\n"
    "ds_read_b64_tr_b16 v[206:207], v246 offset:8704\n"
    "s_waitcnt lgkmcnt(8)\n"
    "v_mfma_f32_32x32x16_bf16 v[160:175], v[184:187], v[16:19], v[160:175]\n"
    "s_add_u32 m0, s48, 0x16000\n"
    "s_add_u32 s49, s57, 0x80\n"
    "buffer_load_dwordx4 v226, s[24:27], s49 offen lds\n"
    "v_mfma_f32_32x32x16_bf16 v[160:175], v[188:191], v[24:27], v[160:175]\n"
    "s_waitcnt vmcnt(27) lgkmcnt(4)\n"
    "ds_read_b64_tr_b16 v[176:177], v228 offset:40960\n"
    "ds_read_b64_tr_b16 v[178:179], v228 offset:41472\n"
    "v_mfma_f32_32x32x16_bf16 v[96:111], v[192:195], a[0:3], 0\n"
    "buffer_load_dwordx4 v227, s[24:27], s49 offen offset:1024 lds\n"
    "v_mfma_f32_32x32x16_bf16 v[112:127], v[192:195], a[4:7], 0\n"
    "v_mul_f32 v208, s51, v32\n"
    "v_mul_f32 v209, s51, v33\n"
    "v_mul_f32 v210, s51, v34\n"
    "v_mul_f32 v211, s51, v35\n"
    "v_exp_f32 v208, v208\n"
    "ds_read_b64_tr_b16 v[180:181], v232 offset:40960\n"
    "ds_read_b64_tr_b16 v[182:183], v232 offset:41472\n"
    "v_mfma_f32_32x32x16_bf16 v[128:143], v[196:199], a[0:3], 0\n"
    "s_add_u32 m0, s48, 0x2000\n"
    "s_add_u32 s49, s57, 0x24080\n"
    "buffer_load_dwordx4 v226, s[24:27], s49 offen lds\n"
    "v_exp_f32 v209, v209\n"
    "v_exp_f32 v210, v210\n"
    "v_mfma_f32_32x32x16_bf16 v[144:159], v[196:199], a[4:7], 0\n"
    "v_exp_f32 v211, v211\n"
    "v_add_f32 v208, 1.0, v208\n"
    "v_add_f32 v209, 1.0, v209\n"
    "v_add_f32 v210, 1.0, v210\n"
    "v_add_f32 v211, 1.0, v211\n"
    "s_waitcnt vmcnt(27) lgkmcnt(4)\n"
    "ds_read_b64_tr_b16 v[184:185], v228 offset:49152\n"
    "ds_read_b64_tr_b16 v[186:187], v228 offset:49664\n"
    "v_mfma_f32_32x32x16_bf16 v[96:111], v[200:203], a[8:11], v[96:111]\n"
    "buffer_load_dwordx4 v227, s[24:27], s49 offen offset:1024 lds\n"
    "v_rcp_f32 v208, v208\n"
    "v_rcp_f32 v209, v209\n"
    "v_mfma_f32_32x32x16_bf16 v[112:127], v[200:203], a[12:15], v[112:127]\n"
    "v_rcp_f32 v210, v210\n"
    "v_rcp_f32 v211, v211\n"
    "v_mul_f32 v208, v32, v208\n"
    "v_mul_f32 v209, v33, v209\n"
    "ds_read_b64_tr_b16 v[188:189], v232 offset:49152\n"
    "ds_read_b64_tr_b16 v[190:191], v232 offset:49664\n"
    "v_mfma_f32_32x32x16_bf16 v[128:143], v[204:207], a[8:11], v[128:143]\n"
    "s_add_u32 m0, s48, 0x12000\n"
    "s_add_u32 s49, s57, 0x48080\n"
    "buffer_load_dwordx4 v226, s[24:27], s49 offen lds\n"
    "v_mul_f32 v210, v34, v210\n"
    "v_mul_f32 v211, v35, v211\n"
    "v_cvt_pk_bf16_f32 v32, v208, v209\n"
    "v_cvt_pk_bf16_f32 v33, v210, v211\n"
    "v_mfma_f32_32x32x16_bf16 v[144:159], v[204:207], a[12:15], v[144:159]\n"
    "v_mul_f32 v212, s51, v36\n"
    "v_mul_f32 v213, s51, v37\n"
    "v_mul_f32 v214, s51, v38\n"
    "v_mul_f32 v215, s51, v39\n"
    "v_exp_f32 v212, v212\n"
    "s_waitcnt vmcnt(27) lgkmcnt(4)\n"
    "ds_read_b64_tr_b16 v[192:193], v229 offset:57344\n"
    "ds_read_b64_tr_b16 v[194:195], v229 offset:57856\n"
    "v_mfma_f32_32x32x16_bf16 v[96:111], v[176:179], a[16:19], v[96:111]\n"
    "buffer_load_dwordx4 v227, s[24:27], s49 offen offset:1024 lds\n"
    "v_exp_f32 v213, v213\n"
    "v_exp_f32 v214, v214\n"
    "v_mfma_f32_32x32x16_bf16 v[112:127], v[176:179], a[20:23], v[112:127]\n"
    "v_exp_f32 v215, v215\n"
    "v_add_f32 v212, 1.0, v212\n"
    "v_add_f32 v213, 1.0, v213\n"
    "v_add_f32 v214, 1.0, v214\n"
    "v_add_f32 v215, 1.0, v215\n"
    "ds_read_b64_tr_b16 v[196:197], v246 offset:57344\n"
    "ds_read_b64_tr_b16 v[198:199], v246 offset:57856\n"
    "v_mfma_f32_32x32x16_bf16 v[128:143], v[180:183], a[16:19], v[128:143]\n"
    "s_add_u32 m0, s48, 0xa000\n"
    "s_add_u32 s49, s57, 0x6c080\n"
    "buffer_load_dwordx4 v226, s[24:27], s49 offen lds\n"
    "v_rcp_f32 v212, v212\n"
    "v_rcp_f32 v213, v213\n"
    "v_mfma_f32_32x32x16_bf16 v[144:159], v[180:183], a[20:23], v[144:159]\n"
    "v_rcp_f32 v214, v214\n"
    "v_rcp_f32 v215, v215\n"
    "v_mul_f32 v212, v36, v212\n"
    "v_mul_f32 v213, v37, v213\n"
    "s_waitcnt vmcnt(27) lgkmcnt(4)\n"
    "ds_read_b64_tr_b16 v[200:201], v228 offset:16384\n"
    "ds_read_b64_tr_b16 v[202:203], v228 offset:16896\n"
    "v_mfma_f32_32x32x16_bf16 v[96:111], v[184:187], a[24:27], v[96:111]\n"
    "buffer_load_dwordx4 v227, s[24:27], s49 offen offset:1024 lds\n"
    "v_mul_f32 v214, v38, v214\n"
    "v_mul_f32 v215, v39, v215\n"
    "v_cvt_pk_bf16_f32 v34, v212, v213\n"
    "v_cvt_pk_bf16_f32 v35, v214, v215\n"
    "v_mfma_f32_32x32x16_bf16 v[112:127], v[184:187], a[28:31], v[112:127]\n"
    "v_mul_f32 v208, s51, v40\n"
    "v_mul_f32 v209, s51, v41\n"
    "v_mul_f32 v210, s51, v42\n"
    "v_mul_f32 v211, s51, v43\n"
    "v_exp_f32 v208, v208\n"
    "ds_read_b64_tr_b16 v[204:205], v232 offset:16384\n"
    "ds_read_b64_tr_b16 v[206:207], v232 offset:16896\n"
    "v_mfma_f32_32x32x16_bf16 v[128:143], v[188:191], a[24:27], v[128:143]\n"
    "s_add_u32 m0, s48, 0xc000\n"
    "s_add_u32 s49, s58, 0x80\n"
    "buffer_load_dwordx4 v226, s[24:27], s49 offen lds\n"
    "v_exp_f32 v209, v209\n"
    "v_exp_f32 v210, v210\n"
    "v_mfma_f32_32x32x16_bf16 v[144:159], v[188:191], a[28:31], v[144:159]\n"
    "v_exp_f32 v211, v211\n"
    "v_add_f32 v208, 1.0, v208\n"
    "v_add_f32 v209, 1.0, v209\n"
    "v_add_f32 v210, 1.0, v210\n"
    "v_add_f32 v211, 1.0, v211\n"
    "s_waitcnt vmcnt(27) lgkmcnt(4)\n"
    "ds_read_b64_tr_b16 v[176:177], v228 offset:24576\n"
    "ds_read_b64_tr_b16 v[178:179], v228 offset:25088\n"
    "v_mfma_f32_32x32x16_bf16 v[96:111], v[192:195], a[32:35], v[96:111]\n"
    "buffer_load_dwordx4 v227, s[24:27], s49 offen offset:1024 lds\n"
    "v_rcp_f32 v208, v208\n"
    "v_rcp_f32 v209, v209\n"
    "v_mfma_f32_32x32x16_bf16 v[112:127], v[192:195], a[36:39], v[112:127]\n"
    "v_rcp_f32 v210, v210\n"
    "v_rcp_f32 v211, v211\n"
    "v_mul_f32 v208, v40, v208\n"
    "v_mul_f32 v209, v41, v209\n"
    "ds_read_b64_tr_b16 v[180:181], v232 offset:24576\n"
    "ds_read_b64_tr_b16 v[182:183], v232 offset:25088\n"
    "v_mfma_f32_32x32x16_bf16 v[128:143], v[196:199], a[32:35], v[128:143]\n"
    "s_add_u32 m0, s48, 0x1e000\n"
    "s_add_u32 s49, s58, 0x24080\n"
    "buffer_load_dwordx4 v226, s[24:27], s49 offen lds\n"
    "v_mul_f32 v210, v42, v210\n"
    "v_mul_f32 v211, v43, v211\n"
    "v_cvt_pk_bf16_f32 v36, v208, v209\n"
    "v_cvt_pk_bf16_f32 v37, v210, v211\n"
    "v_mfma_f32_32x32x16_bf16 v[144:159], v[196:199], a[36:39], v[144:159]\n"
    "v_mul_f32 v212, s51, v44\n"
    "v_mul_f32 v213, s51, v45\n"
    "v_mul_f32 v214, s51, v46\n"
    "v_mul_f32 v215, s51, v47\n"
    "v_exp_f32 v212, v212\n"
    "s_waitcnt vmcnt(27) lgkmcnt(4)\n"
    "ds_read_b64_tr_b16 v[184:185], v229 offset:32768\n"
    "ds_read_b64_tr_b16 v[186:187], v229 offset:33280\n"
    "v_mfma_f32_32x32x16_bf16 v[96:111], v[200:203], a[40:43], v[96:111]\n"
    "buffer_load_dwordx4 v227, s[24:27], s49 offen offset:1024 lds\n"
    "s_lshr_b32 s50, s9, 3\n"
    "s_lshl_b32 s50, s50, 2\n"
    "s_add_u32 s50, s50, s44\n"
    "s_mul_i32 s50, s50, 0x2880\n"
    "s_lshr_b32 s9, s46, 1\n"
    "s_mul_i32 s9, s9, 0x144000\n"
    "s_add_u32 s9, s9, s50\n"
    "v_mul_u32_u24 v250, 64, v244\n"
    "v_lshrrev_b32 v250, 6, v250\n"
    "v_lshlrev_b32 v250, 7, v250\n"
    "buffer_load_dword v249, v250, s[28:31], s9 offen offset:0\n"
    "v_exp_f32 v213, v213\n"
    "v_exp_f32 v214, v214\n"
    "v_mfma_f32_32x32x16_bf16 v[112:127], v[200:203], a[44:47], v[112:127]\n"
    "v_exp_f32 v215, v215\n"
    "v_add_f32 v212, 1.0, v212\n"
    "v_add_f32 v213, 1.0, v213\n"
    "v_add_f32 v214, 1.0, v214\n"
    "v_add_f32 v215, 1.0, v215\n"
    "ds_read_b64_tr_b16 v[188:189], v246 offset:32768\n"
    "ds_read_b64_tr_b16 v[190:191], v246 offset:33280\n"
    "v_mfma_f32_32x32x16_bf16 v[128:143], v[204:207], a[40:43], v[128:143]\n"
    "s_add_u32 m0, s48, 0x4000\n"
    "s_add_u32 s49, s58, 0x48080\n"
    "buffer_load_dwordx4 v226, s[24:27], s49 offen lds\n"
    "v_rcp_f32 v212, v212\n"
    "v_rcp_f32 v213, v213\n"
    "v_mfma_f32_32x32x16_bf16 v[144:159], v[204:207], a[44:47], v[144:159]\n"
    "v_rcp_f32 v214, v214\n"
    "v_rcp_f32 v215, v215\n"
    "v_mul_f32 v212, v44, v212\n"
    "v_mul_f32 v213, v45, v213\n"
    "s_waitcnt vmcnt(28) lgkmcnt(4)\n"
    "ds_read_b64_tr_b16 v[192:193], v229 offset:40960\n"
    "ds_read_b64_tr_b16 v[194:195], v229 offset:41472\n"
    "v_mfma_f32_32x32x16_bf16 v[96:111], v[176:179], a[48:51], v[96:111]\n"
    "buffer_load_dwordx4 v227, s[24:27], s49 offen offset:1024 lds\n"
    "v_mul_f32 v214, v46, v214\n"
    "v_mul_f32 v215, v47, v215\n"
    "v_cvt_pk_bf16_f32 v38, v212, v213\n"
    "v_cvt_pk_bf16_f32 v39, v214, v215\n"
    "ds_write_b128 v236, v[32:35] offset:0\n"
    "ds_write_b128 v236, v[36:39] offset:1024\n"
    "v_mfma_f32_32x32x16_bf16 v[112:127], v[176:179], a[52:55], v[112:127]\n"
    "v_mul_f32 v208, s51, v48\n"
    "v_mul_f32 v209, s51, v49\n"
    "v_mul_f32 v210, s51, v50\n"
    "v_mul_f32 v211, s51, v51\n"
    "v_exp_f32 v208, v208\n"
    "ds_read_b64_tr_b16 v[196:197], v246 offset:40960\n"
    "ds_read_b64_tr_b16 v[198:199], v246 offset:41472\n"
    "v_mfma_f32_32x32x16_bf16 v[128:143], v[180:183], a[48:51], v[128:143]\n"
    "s_add_u32 m0, s48, 0x6000\n"
    "s_add_u32 s49, s58, 0x6c080\n"
    "buffer_load_dwordx4 v226, s[24:27], s49 offen lds\n"
    "v_exp_f32 v209, v209\n"
    "v_exp_f32 v210, v210\n"
    "v_mfma_f32_32x32x16_bf16 v[144:159], v[180:183], a[52:55], v[144:159]\n"
    "v_exp_f32 v211, v211\n"
    "v_add_f32 v208, 1.0, v208\n"
    "v_add_f32 v209, 1.0, v209\n"
    "v_add_f32 v210, 1.0, v210\n"
    "v_add_f32 v211, 1.0, v211\n"
    "s_waitcnt vmcnt(28) lgkmcnt(6)\n"
    "ds_read_b64_tr_b16 v[200:201], v231 offset:0\n"
    "ds_read_b64_tr_b16 v[202:203], v231 offset:512\n"
    "v_mfma_f32_32x32x16_bf16 v[96:111], v[184:187], a[56:59], v[96:111]\n"
    "buffer_load_dwordx4 v227, s[24:27], s49 offen offset:1024 lds\n"
    "v_rcp_f32 v208, v208\n"
    "v_rcp_f32 v209, v209\n"
    "v_mfma_f32_32x32x16_bf16 v[112:127], v[184:187], a[60:63], v[112:127]\n"
    "v_rcp_f32 v210, v210\n"
    "v_rcp_f32 v211, v211\n"
    "v_mul_f32 v208, v48, v208\n"
    "v_mul_f32 v209, v49, v209\n"
    "ds_read_b64_tr_b16 v[204:205], v247 offset:0\n"
    "ds_read_b64_tr_b16 v[206:207], v247 offset:512\n"
    "v_mfma_f32_32x32x16_bf16 v[128:143], v[188:191], a[56:59], v[128:143]\n"
    "s_add_u32 m0, s48, 0x18000\n"
    "s_add_u32 s49, s59, 0x80\n"
    "buffer_load_dwordx4 v226, s[24:27], s49 offen lds\n"
    "v_mul_f32 v210, v50, v210\n"
    "v_mul_f32 v211, v51, v211\n"
    "v_cvt_pk_bf16_f32 v48, v208, v209\n"
    "v_cvt_pk_bf16_f32 v49, v210, v211\n"
    "v_mfma_f32_32x32x16_bf16 v[144:159], v[188:191], a[60:63], v[144:159]\n"
    "v_mul_f32 v212, s51, v52\n"
    "v_mul_f32 v213, s51, v53\n"
    "v_mul_f32 v214, s51, v54\n"
    "v_mul_f32 v215, s51, v55\n"
    "v_exp_f32 v212, v212\n"
    "s_waitcnt vmcnt(28) lgkmcnt(4)\n"
    "ds_read_b64_tr_b16 v[176:177], v228 offset:32768\n"
    "ds_read_b64_tr_b16 v[178:179], v228 offset:33280\n"
    "v_mfma_f32_32x32x16_bf16 v[96:111], v[192:195], a[64:67], v[96:111]\n"
    "buffer_load_dwordx4 v227, s[24:27], s49 offen offset:1024 lds\n"
    "v_exp_f32 v213, v213\n"
    "v_exp_f32 v214, v214\n"
    "v_mfma_f32_32x32x16_bf16 v[112:127], v[192:195], a[68:71], v[112:127]\n"
    "v_exp_f32 v215, v215\n"
    "v_add_f32 v212, 1.0, v212\n"
    "v_add_f32 v213, 1.0, v213\n"
    "v_add_f32 v214, 1.0, v214\n"
    "v_add_f32 v215, 1.0, v215\n"
    "ds_read_b64_tr_b16 v[180:181], v232 offset:32768\n"
    "ds_read_b64_tr_b16 v[182:183], v232 offset:33280\n"
    "v_mfma_f32_32x32x16_bf16 v[128:143], v[196:199], a[64:67], v[128:143]\n"
    "s_add_u32 m0, s48, 0x1a000\n"
    "s_add_u32 s49, s59, 0x24080\n"
    "buffer_load_dwordx4 v226, s[24:27], s49 offen lds\n"
    "v_rcp_f32 v212, v212\n"
    "v_rcp_f32 v213, v213\n"
    "v_mfma_f32_32x32x16_bf16 v[144:159], v[196:199], a[68:71], v[144:159]\n"
    "v_rcp_f32 v214, v214\n"
    "v_rcp_f32 v215, v215\n"
    "v_mul_f32 v212, v52, v212\n"
    "v_mul_f32 v213, v53, v213\n"
    "s_waitcnt vmcnt(28) lgkmcnt(4)\n"
    "ds_read_b64_tr_b16 v[184:185], v228 offset:57344\n"
    "ds_read_b64_tr_b16 v[186:187], v228 offset:57856\n"
    "v_mfma_f32_32x32x16_bf16 v[96:111], v[200:203], a[72:75], v[96:111]\n"
    "buffer_load_dwordx4 v227, s[24:27], s49 offen offset:1024 lds\n"
    "v_mul_f32 v214, v54, v214\n"
    "v_mul_f32 v215, v55, v215\n"
    "v_cvt_pk_bf16_f32 v50, v212, v213\n"
    "v_cvt_pk_bf16_f32 v51, v214, v215\n"
    "v_mfma_f32_32x32x16_bf16 v[112:127], v[200:203], a[76:79], v[112:127]\n"
    "v_mul_f32 v208, s51, v56\n"
    "v_mul_f32 v209, s51, v57\n"
    "v_mul_f32 v210, s51, v58\n"
    "v_mul_f32 v211, s51, v59\n"
    "v_exp_f32 v208, v208\n"
    "ds_read_b64_tr_b16 v[188:189], v232 offset:57344\n"
    "ds_read_b64_tr_b16 v[190:191], v232 offset:57856\n"
    "v_mfma_f32_32x32x16_bf16 v[128:143], v[204:207], a[72:75], v[128:143]\n"
    "s_add_u32 m0, s48, 0x20000\n"
    "s_add_u32 s49, s59, 0x48080\n"
    "buffer_load_dwordx4 v226, s[24:27], s49 offen lds\n"
    "v_exp_f32 v209, v209\n"
    "v_exp_f32 v210, v210\n"
    "v_mfma_f32_32x32x16_bf16 v[144:159], v[204:207], a[76:79], v[144:159]\n"
    "v_exp_f32 v211, v211\n"
    "v_add_f32 v208, 1.0, v208\n"
    "v_add_f32 v209, 1.0, v209\n"
    "v_add_f32 v210, 1.0, v210\n"
    "v_add_f32 v211, 1.0, v211\n"
    "s_waitcnt vmcnt(28) lgkmcnt(4)\n"
    "ds_read_b64_tr_b16 v[192:193], v229 offset:16384\n"
    "ds_read_b64_tr_b16 v[194:195], v229 offset:16896\n"
    "v_mfma_f32_32x32x16_bf16 v[96:111], v[176:179], a[80:83], v[96:111]\n"
    "buffer_load_dwordx4 v227, s[24:27], s49 offen offset:1024 lds\n"
    "v_rcp_f32 v208, v208\n"
    "v_rcp_f32 v209, v209\n"
    "v_mfma_f32_32x32x16_bf16 v[112:127], v[176:179], a[84:87], v[112:127]\n"
    "v_rcp_f32 v210, v210\n"
    "v_rcp_f32 v211, v211\n"
    "v_mul_f32 v208, v56, v208\n"
    "v_mul_f32 v209, v57, v209\n"
    "ds_read_b64_tr_b16 v[196:197], v246 offset:16384\n"
    "ds_read_b64_tr_b16 v[198:199], v246 offset:16896\n"
    "v_mfma_f32_32x32x16_bf16 v[128:143], v[180:183], a[80:83], v[128:143]\n"
    "s_add_u32 m0, s48, 0x8000\n"
    "s_add_u32 s49, s59, 0x6c080\n"
    "buffer_load_dwordx4 v226, s[24:27], s49 offen lds\n"
    "v_mul_f32 v210, v58, v210\n"
    "v_mul_f32 v211, v59, v211\n"
    "v_cvt_pk_bf16_f32 v52, v208, v209\n"
    "v_cvt_pk_bf16_f32 v53, v210, v211\n"
    "v_mfma_f32_32x32x16_bf16 v[144:159], v[180:183], a[84:87], v[144:159]\n"
    "v_mul_f32 v212, s51, v60\n"
    "v_mul_f32 v213, s51, v61\n"
    "v_mul_f32 v214, s51, v62\n"
    "v_mul_f32 v215, s51, v63\n"
    "v_exp_f32 v212, v212\n"
    "s_waitcnt vmcnt(28) lgkmcnt(4)\n"
    "ds_read_b64_tr_b16 v[200:201], v229 offset:49152\n"
    "ds_read_b64_tr_b16 v[202:203], v229 offset:49664\n"
    "v_mfma_f32_32x32x16_bf16 v[96:111], v[184:187], a[88:91], v[96:111]\n"
    "buffer_load_dwordx4 v227, s[24:27], s49 offen offset:1024 lds\n"
    "v_exp_f32 v213, v213\n"
    "v_exp_f32 v214, v214\n"
    "v_mfma_f32_32x32x16_bf16 v[112:127], v[184:187], a[92:95], v[112:127]\n"
    "v_exp_f32 v215, v215\n"
    "v_add_f32 v212, 1.0, v212\n"
    "v_add_f32 v213, 1.0, v213\n"
    "v_add_f32 v214, 1.0, v214\n"
    "v_add_f32 v215, 1.0, v215\n"
    "ds_read_b64_tr_b16 v[204:205], v246 offset:49152\n"
    "ds_read_b64_tr_b16 v[206:207], v246 offset:49664\n"
    "v_mfma_f32_32x32x16_bf16 v[128:143], v[188:191], a[88:91], v[128:143]\n"
    "s_add_u32 m0, s48, 0xe000\n"
    "s_add_u32 s49, s60, 0x80\n"
    "buffer_load_dwordx4 v226, s[24:27], s49 offen lds\n"
    "v_rcp_f32 v212, v212\n"
    "v_rcp_f32 v213, v213\n"
    "v_mfma_f32_32x32x16_bf16 v[144:159], v[188:191], a[92:95], v[144:159]\n"
    "v_rcp_f32 v214, v214\n"
    "v_rcp_f32 v215, v215\n"
    "v_mul_f32 v212, v60, v212\n"
    "v_mul_f32 v213, v61, v213\n"
    "s_waitcnt vmcnt(28) lgkmcnt(4)\n"
    "ds_read_b64_tr_b16 v[176:177], v228 offset:0\n"
    "ds_read_b64_tr_b16 v[178:179], v228 offset:512\n"
    "v_mfma_f32_32x32x16_bf16 v[96:111], v[192:195], a[96:99], v[96:111]\n"
    "buffer_load_dwordx4 v227, s[24:27], s49 offen offset:1024 lds\n"
    "buffer_load_dword v249, v250, s[28:31], s9 offen offset:2176\n"
    "v_mul_f32 v214, v62, v214\n"
    "v_mul_f32 v215, v63, v215\n"
    "v_cvt_pk_bf16_f32 v54, v212, v213\n"
    "v_cvt_pk_bf16_f32 v55, v214, v215\n"
    "ds_write_b128 v237, v[48:51] offset:0\n"
    "ds_write_b128 v237, v[52:55] offset:1024\n"
    "v_mfma_f32_32x32x16_bf16 v[112:127], v[192:195], a[100:103], v[112:127]\n"
    "v_mul_f32 v208, s51, v64\n"
    "v_mul_f32 v209, s51, v65\n"
    "v_mul_f32 v210, s51, v66\n"
    "v_mul_f32 v211, s51, v67\n"
    "v_exp_f32 v208, v208\n"
    "ds_read_b64_tr_b16 v[180:181], v232 offset:0\n"
    "ds_read_b64_tr_b16 v[182:183], v232 offset:512\n"
    "v_mfma_f32_32x32x16_bf16 v[128:143], v[196:199], a[96:99], v[128:143]\n"
    "s_add_u32 m0, s48, 0x14000\n"
    "s_add_u32 s49, s60, 0x24080\n"
    "buffer_load_dwordx4 v226, s[24:27], s49 offen lds\n"
    "v_exp_f32 v209, v209\n"
    "v_exp_f32 v210, v210\n"
    "v_mfma_f32_32x32x16_bf16 v[144:159], v[196:199], a[100:103], v[144:159]\n"
    "v_exp_f32 v211, v211\n"
    "v_add_f32 v208, 1.0, v208\n"
    "v_add_f32 v209, 1.0, v209\n"
    "v_add_f32 v210, 1.0, v210\n"
    "v_add_f32 v211, 1.0, v211\n"
    "s_waitcnt vmcnt(29) lgkmcnt(6)\n"
    "ds_read_b64_tr_b16 v[184:185], v229 offset:0\n"
    "ds_read_b64_tr_b16 v[186:187], v229 offset:512\n"
    "v_mfma_f32_32x32x16_bf16 v[96:111], v[200:203], a[104:107], v[96:111]\n"
    "buffer_load_dwordx4 v227, s[24:27], s49 offen offset:1024 lds\n"
    "v_rcp_f32 v208, v208\n"
    "v_rcp_f32 v209, v209\n"
    "s_waitcnt lgkmcnt(4)\n"
    "s_barrier\n"
    "v_mfma_f32_32x32x16_bf16 v[112:127], v[200:203], a[108:111], v[112:127]\n"
    "v_rcp_f32 v210, v210\n"
    "v_rcp_f32 v211, v211\n"
    "v_mul_f32 v208, v64, v208\n"
    "v_mul_f32 v209, v65, v209\n"
    "ds_read_b128 a[0:3], v234 offset:0\n"
    "ds_read_b128 a[4:7], v235 offset:0\n"
    "ds_read_b64_tr_b16 v[188:189], v246 offset:0\n"
    "ds_read_b64_tr_b16 v[190:191], v246 offset:512\n"
    "v_mfma_f32_32x32x16_bf16 v[128:143], v[204:207], a[104:107], v[128:143]\n"
    "s_add_u32 m0, s48, 0x1c000\n"
    "s_add_u32 s49, s60, 0x48080\n"
    "buffer_load_dwordx4 v226, s[24:27], s49 offen lds\n"
    "v_mul_f32 v210, v66, v210\n"
    "v_mul_f32 v211, v67, v211\n"
    "v_cvt_pk_bf16_f32 v64, v208, v209\n"
    "v_cvt_pk_bf16_f32 v65, v210, v211\n"
    "ds_read_b128 a[8:11], v234 offset:1024\n"
    "ds_read_b128 a[12:15], v235 offset:1024\n"
    "v_mfma_f32_32x32x16_bf16 v[144:159], v[204:207], a[108:111], v[144:159]\n"
    "v_mul_f32 v212, s51, v68\n"
    "v_mul_f32 v213, s51, v69\n"
    "v_mul_f32 v214, s51, v70\n"
    "v_mul_f32 v215, s51, v71\n"
    "v_exp_f32 v212, v212\n"
    "ds_read_b128 a[16:19], v234 offset:2048\n"
    "ds_read_b128 a[20:23], v235 offset:2048\n"
    "s_waitcnt vmcnt(29) lgkmcnt(10)\n"
    "ds_read_b64_tr_b16 v[192:193], v229 offset:24576\n"
    "ds_read_b64_tr_b16 v[194:195], v229 offset:25088\n"
    "v_mfma_f32_32x32x16_bf16 v[96:111], v[176:179], a[112:115], v[96:111]\n"
    "buffer_load_dwordx4 v227, s[24:27], s49 offen offset:1024 lds\n"
    "v_exp_f32 v213, v213\n"
    "v_exp_f32 v214, v214\n"
    "ds_read_b128 a[24:27], v234 offset:3072\n"
    "ds_read_b128 a[28:31], v235 offset:3072\n"
    "v_mfma_f32_32x32x16_bf16 v[112:127], v[176:179], a[116:119], v[112:127]\n"
    "v_exp_f32 v215, v215\n"
    "v_add_f32 v212, 1.0, v212\n"
    "v_add_f32 v213, 1.0, v213\n"
    "v_add_f32 v214, 1.0, v214\n"
    "v_add_f32 v215, 1.0, v215\n"
    "ds_read_b128 a[32:35], v234 offset:4096\n"
    "ds_read_b128 a[36:39], v235 offset:4096\n"
    "ds_read_b64_tr_b16 v[196:197], v246 offset:24576\n"
    "ds_read_b64_tr_b16 v[198:199], v246 offset:25088\n"
    "v_mfma_f32_32x32x16_bf16 v[128:143], v[180:183], a[112:115], v[128:143]\n"
    "s_add_u32 m0, s48, 0x22000\n"
    "s_add_u32 s49, s60, 0x6c080\n"
    "buffer_load_dwordx4 v226, s[24:27], s49 offen lds\n"
    "v_rcp_f32 v212, v212\n"
    "v_rcp_f32 v213, v213\n"
    "ds_read_b128 a[40:43], v234 offset:5120\n"
    "ds_read_b128 a[44:47], v235 offset:5120\n"
    "v_mfma_f32_32x32x16_bf16 v[144:159], v[180:183], a[116:119], v[144:159]\n"
    "v_rcp_f32 v214, v214\n"
    "v_rcp_f32 v215, v215\n"
    "v_mul_f32 v212, v68, v212\n"
    "v_mul_f32 v213, v69, v213\n"
    "ds_read_b128 a[48:51], v234 offset:6144\n"
    "ds_read_b128 a[52:55], v235 offset:6144\n"
    "s_waitcnt vmcnt(29) lgkmcnt(13)\n"
    "ds_read_b64_tr_b16 v[200:201], v228 offset:8192\n"
    "ds_read_b64_tr_b16 v[202:203], v228 offset:8704\n"
    "v_mfma_f32_32x32x16_bf16 v[96:111], v[184:187], a[120:123], v[96:111]\n"
    "buffer_load_dwordx4 v227, s[24:27], s49 offen offset:1024 lds\n"
    "v_mul_f32 v214, v70, v214\n"
    "v_mul_f32 v215, v71, v215\n"
    "v_cvt_pk_bf16_f32 v66, v212, v213\n"
    "v_cvt_pk_bf16_f32 v67, v214, v215\n"
    "ds_read_b128 a[56:59], v234 offset:7168\n"
    "ds_read_b128 a[60:63], v235 offset:7168\n"
    "v_mfma_f32_32x32x16_bf16 v[112:127], v[184:187], a[124:127], v[112:127]\n"
    "v_mul_f32 v208, s51, v72\n"
    "v_mul_f32 v209, s51, v73\n"
    "v_mul_f32 v210, s51, v74\n"
    "v_mul_f32 v211, s51, v75\n"
    "v_exp_f32 v208, v208\n"
    "ds_read_b64_tr_b16 v[204:205], v232 offset:8192\n"
    "ds_read_b64_tr_b16 v[206:207], v232 offset:8704\n"
    "v_mfma_f32_32x32x16_bf16 v[128:143], v[188:191], a[120:123], v[128:143]\n"
    "s_add_u32 m0, s48, 0\n"
    "s_add_u32 s49, s61, 0x80\n"
    "buffer_load_dwordx4 v226, s[24:27], s49 offen lds\n"
    "v_exp_f32 v209, v209\n"
    "v_exp_f32 v210, v210\n"
    "v_mfma_f32_32x32x16_bf16 v[144:159], v[188:191], a[124:127], v[144:159]\n"
    "v_exp_f32 v211, v211\n"
    "v_add_f32 v208, 1.0, v208\n"
    "v_add_f32 v209, 1.0, v209\n"
    "v_add_f32 v210, 1.0, v210\n"
    "v_add_f32 v211, 1.0, v211\n"
    "s_waitcnt vmcnt(29) lgkmcnt(10)\n"
    "ds_read_b64_tr_b16 v[176:177], v229 offset:8192\n"
    "ds_read_b64_tr_b16 v[178:179], v229 offset:8704\n"
    "v_mfma_f32_32x32x16_bf16 v[96:111], v[192:195], a[128:131], v[96:111]\n"
    "buffer_load_dwordx4 v227, s[24:27], s49 offen offset:1024 lds\n"
    "v_rcp_f32 v208, v208\n"
    "v_rcp_f32 v209, v209\n"
    "v_mfma_f32_32x32x16_bf16 v[112:127], v[192:195], a[132:135], v[112:127]\n"
    "v_rcp_f32 v210, v210\n"
    "v_rcp_f32 v211, v211\n"
    "v_mul_f32 v208, v72, v208\n"
    "v_mul_f32 v209, v73, v209\n"
    "ds_read_b64_tr_b16 v[180:181], v246 offset:8192\n"
    "ds_read_b64_tr_b16 v[182:183], v246 offset:8704\n"
    "v_mfma_f32_32x32x16_bf16 v[128:143], v[196:199], a[128:131], v[128:143]\n"
    "s_add_u32 m0, s48, 0x10000\n"
    "s_add_u32 s49, s61, 0x24080\n"
    "buffer_load_dwordx4 v226, s[24:27], s49 offen lds\n"
    "v_mul_f32 v210, v74, v210\n"
    "v_mul_f32 v211, v75, v211\n"
    "v_cvt_pk_bf16_f32 v68, v208, v209\n"
    "v_cvt_pk_bf16_f32 v69, v210, v211\n"
    "s_waitcnt lgkmcnt(6)\n"
    "s_barrier\n"
    "v_mfma_f32_32x32x16_bf16 v[144:159], v[196:199], a[132:135], v[144:159]\n"
    "v_mul_f32 v212, s51, v76\n"
    "v_mul_f32 v213, s51, v77\n"
    "v_mul_f32 v214, s51, v78\n"
    "v_mul_f32 v215, s51, v79\n"
    "v_exp_f32 v212, v212\n"
    "s_waitcnt vmcnt(29) lgkmcnt(4)\n"
    "ds_read_b64_tr_b16 v[184:185], v228 offset:40960\n"
    "ds_read_b64_tr_b16 v[186:187], v228 offset:41472\n"
    "v_mfma_f32_32x32x16_bf16 v[96:111], v[200:203], a[136:139], v[96:111]\n"
    "buffer_load_dwordx4 v227, s[24:27], s49 offen offset:1024 lds\n"
    "v_exp_f32 v213, v213\n"
    "v_exp_f32 v214, v214\n"
    "v_mfma_f32_32x32x16_bf16 v[112:127], v[200:203], a[140:143], v[112:127]\n"
    "v_exp_f32 v215, v215\n"
    "v_add_f32 v212, 1.0, v212\n"
    "v_add_f32 v213, 1.0, v213\n"
    "v_add_f32 v214, 1.0, v214\n"
    "v_add_f32 v215, 1.0, v215\n"
    "ds_read_b64_tr_b16 v[188:189], v232 offset:40960\n"
    "ds_read_b64_tr_b16 v[190:191], v232 offset:41472\n"
    "v_mfma_f32_32x32x16_bf16 v[128:143], v[204:207], a[136:139], v[128:143]\n"
    "s_add_u32 m0, s48, 0x16000\n"
    "s_add_u32 s49, s61, 0x48080\n"
    "buffer_load_dwordx4 v226, s[24:27], s49 offen lds\n"
    "v_rcp_f32 v212, v212\n"
    "v_rcp_f32 v213, v213\n"
    "v_mfma_f32_32x32x16_bf16 v[144:159], v[204:207], a[140:143], v[144:159]\n"
    "v_rcp_f32 v214, v214\n"
    "v_rcp_f32 v215, v215\n"
    "v_mul_f32 v212, v76, v212\n"
    "v_mul_f32 v213, v77, v213\n"
    "s_waitcnt vmcnt(29) lgkmcnt(4)\n"
    "ds_read_b64_tr_b16 v[192:193], v228 offset:49152\n"
    "ds_read_b64_tr_b16 v[194:195], v228 offset:49664\n"
    "v_mfma_f32_32x32x16_bf16 v[96:111], v[176:179], a[144:147], v[96:111]\n"
    "buffer_load_dwordx4 v227, s[24:27], s49 offen offset:1024 lds\n"
    "v_mul_f32 v214, v78, v214\n"
    "v_mul_f32 v215, v79, v215\n"
    "v_cvt_pk_bf16_f32 v70, v212, v213\n"
    "v_cvt_pk_bf16_f32 v71, v214, v215\n"
    "ds_write_b128 v236, v[64:67] offset:0\n"
    "ds_write_b128 v236, v[68:71] offset:1024\n"
    "v_mfma_f32_32x32x16_bf16 v[112:127], v[176:179], a[148:151], v[112:127]\n"
    "v_mul_f32 v208, s51, v80\n"
    "v_mul_f32 v209, s51, v81\n"
    "v_mul_f32 v210, s51, v82\n"
    "v_mul_f32 v211, s51, v83\n"
    "v_exp_f32 v208, v208\n"
    "ds_read_b64_tr_b16 v[196:197], v232 offset:49152\n"
    "ds_read_b64_tr_b16 v[198:199], v232 offset:49664\n"
    "v_mfma_f32_32x32x16_bf16 v[128:143], v[180:183], a[144:147], v[128:143]\n"
    "s_add_u32 m0, s48, 0x2000\n"
    "s_add_u32 s49, s61, 0x6c080\n"
    "buffer_load_dwordx4 v226, s[24:27], s49 offen lds\n"
    "v_exp_f32 v209, v209\n"
    "v_exp_f32 v210, v210\n"
    "v_mfma_f32_32x32x16_bf16 v[144:159], v[180:183], a[148:151], v[144:159]\n"
    "v_exp_f32 v211, v211\n"
    "v_add_f32 v208, 1.0, v208\n"
    "v_add_f32 v209, 1.0, v209\n"
    "v_add_f32 v210, 1.0, v210\n"
    "v_add_f32 v211, 1.0, v211\n"
    "s_waitcnt vmcnt(29) lgkmcnt(6)\n"
    "ds_read_b64_tr_b16 v[200:201], v229 offset:57344\n"
    "ds_read_b64_tr_b16 v[202:203], v229 offset:57856\n"
    "v_mfma_f32_32x32x16_bf16 v[96:111], v[184:187], a[152:155], v[96:111]\n"
    "buffer_load_dwordx4 v227, s[24:27], s49 offen offset:1024 lds\n"
    "v_rcp_f32 v208, v208\n"
    "v_rcp_f32 v209, v209\n"
    "v_mfma_f32_32x32x16_bf16 v[112:127], v[184:187], a[156:159], v[112:127]\n"
    "v_rcp_f32 v210, v210\n"
    "v_rcp_f32 v211, v211\n"
    "v_mul_f32 v208, v80, v208\n"
    "v_mul_f32 v209, v81, v209\n"
    "ds_read_b64_tr_b16 v[204:205], v246 offset:57344\n"
    "ds_read_b64_tr_b16 v[206:207], v246 offset:57856\n"
    "v_mfma_f32_32x32x16_bf16 v[128:143], v[188:191], a[152:155], v[128:143]\n"
    "s_add_u32 m0, s48, 0xa000\n"
    "s_add_u32 s49, s46, s44\n"
    "s_and_b32 s49, s49, 7\n"
    "s_cmp_eq_u32 s44, 0\n"
    "s_cselect_b32 s49, 8, s49\n"
    "s_lshl_b32 s49, s49, 7\n"
    "v_add_u32 v224, s49, v241\n"
    "v_add_u32 v225, 0x2000, v224\n"
    "s_add_u32 s49, s63, 0\n"
    "buffer_load_dwordx4 v224, s[28:31], s49 offen lds\n"
    "v_mul_f32 v210, v82, v210\n"
    "v_mul_f32 v211, v83, v211\n"
    "v_cvt_pk_bf16_f32 v80, v208, v209\n"
    "v_cvt_pk_bf16_f32 v81, v210, v211\n"
    "v_mfma_f32_32x32x16_bf16 v[144:159], v[188:191], a[156:159], v[144:159]\n"
    "v_mul_f32 v212, s51, v84\n"
    "v_mul_f32 v213, s51, v85\n"
    "v_mul_f32 v214, s51, v86\n"
    "v_mul_f32 v215, s51, v87\n"
    "v_exp_f32 v212, v212\n"
    "s_waitcnt vmcnt(28) lgkmcnt(4)\n"
    "ds_read_b64_tr_b16 v[176:177], v228 offset:16384\n"
    "ds_read_b64_tr_b16 v[178:179], v228 offset:16896\n"
    "v_mfma_f32_32x32x16_bf16 v[96:111], v[192:195], a[160:163], v[96:111]\n"
    "buffer_load_dwordx4 v225, s[28:31], s49 offen offset:1024 lds\n"
    "v_exp_f32 v213, v213\n"
    "v_exp_f32 v214, v214\n"
    "v_mfma_f32_32x32x16_bf16 v[112:127], v[192:195], a[164:167], v[112:127]\n"
    "v_exp_f32 v215, v215\n"
    "v_add_f32 v212, 1.0, v212\n"
    "v_add_f32 v213, 1.0, v213\n"
    "v_add_f32 v214, 1.0, v214\n"
    "v_add_f32 v215, 1.0, v215\n"
    "ds_read_b64_tr_b16 v[180:181], v232 offset:16384\n"
    "ds_read_b64_tr_b16 v[182:183], v232 offset:16896\n"
    "v_mfma_f32_32x32x16_bf16 v[128:143], v[196:199], a[160:163], v[128:143]\n"
    "s_add_u32 m0, s48, 0xc000\n"
    "s_add_u32 s49, s63, 0x4800\n"
    "buffer_load_dwordx4 v224, s[28:31], s49 offen lds\n"
    "v_rcp_f32 v212, v212\n"
    "v_rcp_f32 v213, v213\n"
    "v_mfma_f32_32x32x16_bf16 v[144:159], v[196:199], a[164:167], v[144:159]\n"
    "v_rcp_f32 v214, v214\n"
    "v_rcp_f32 v215, v215\n"
    "v_mul_f32 v212, v84, v212\n"
    "v_mul_f32 v213, v85, v213\n"
    "s_waitcnt vmcnt(28) lgkmcnt(4)\n"
    "ds_read_b64_tr_b16 v[184:185], v228 offset:24576\n"
    "ds_read_b64_tr_b16 v[186:187], v228 offset:25088\n"
    "v_mfma_f32_32x32x16_bf16 v[96:111], v[200:203], a[168:171], v[96:111]\n"
    "buffer_load_dwordx4 v225, s[28:31], s49 offen offset:1024 lds\n"
    "v_mul_f32 v214, v86, v214\n"
    "v_mul_f32 v215, v87, v215\n"
    "v_cvt_pk_bf16_f32 v82, v212, v213\n"
    "v_cvt_pk_bf16_f32 v83, v214, v215\n"
    "v_mfma_f32_32x32x16_bf16 v[112:127], v[200:203], a[172:175], v[112:127]\n"
    "v_mul_f32 v208, s51, v88\n"
    "v_mul_f32 v209, s51, v89\n"
    "v_mul_f32 v210, s51, v90\n"
    "v_mul_f32 v211, s51, v91\n"
    "v_exp_f32 v208, v208\n"
    "ds_read_b64_tr_b16 v[188:189], v232 offset:24576\n"
    "ds_read_b64_tr_b16 v[190:191], v232 offset:25088\n"
    "v_mfma_f32_32x32x16_bf16 v[128:143], v[204:207], a[168:171], v[128:143]\n"
    "s_add_u32 m0, s48, 0x12000\n"
    "s_add_u32 s49, s63, 0x24000\n"
    "buffer_load_dwordx4 v224, s[28:31], s49 offen lds\n"
    "v_exp_f32 v209, v209\n"
    "v_exp_f32 v210, v210\n"
    "v_mfma_f32_32x32x16_bf16 v[144:159], v[204:207], a[172:175], v[144:159]\n"
    "v_exp_f32 v211, v211\n"
    "v_add_f32 v208, 1.0, v208\n"
    "v_add_f32 v209, 1.0, v209\n"
    "v_add_f32 v210, 1.0, v210\n"
    "v_add_f32 v211, 1.0, v211\n"
    "s_waitcnt vmcnt(28) lgkmcnt(4)\n"
    "ds_read_b64_tr_b16 v[192:193], v229 offset:32768\n"
    "ds_read_b64_tr_b16 v[194:195], v229 offset:33280\n"
    "v_mfma_f32_32x32x16_bf16 v[96:111], v[176:179], a[176:179], v[96:111]\n"
    "buffer_load_dwordx4 v225, s[28:31], s49 offen offset:1024 lds\n"
    "v_rcp_f32 v208, v208\n"
    "v_rcp_f32 v209, v209\n"
    "v_mfma_f32_32x32x16_bf16 v[112:127], v[176:179], a[180:183], v[112:127]\n"
    "v_rcp_f32 v210, v210\n"
    "v_rcp_f32 v211, v211\n"
    "v_mul_f32 v208, v88, v208\n"
    "v_mul_f32 v209, v89, v209\n"
    "ds_read_b64_tr_b16 v[196:197], v246 offset:32768\n"
    "ds_read_b64_tr_b16 v[198:199], v246 offset:33280\n"
    "v_mfma_f32_32x32x16_bf16 v[128:143], v[180:183], a[176:179], v[128:143]\n"
    "s_add_u32 m0, s48, 0x1e000\n"
    "s_add_u32 s49, s63, 0x28800\n"
    "buffer_load_dwordx4 v224, s[28:31], s49 offen lds\n"
    "v_mul_f32 v210, v90, v210\n"
    "v_mul_f32 v211, v91, v211\n"
    "v_cvt_pk_bf16_f32 v84, v208, v209\n"
    "v_cvt_pk_bf16_f32 v85, v210, v211\n"
    "v_mfma_f32_32x32x16_bf16 v[144:159], v[180:183], a[180:183], v[144:159]\n"
    "v_mul_f32 v212, s51, v92\n"
    "v_mul_f32 v213, s51, v93\n"
    "v_mul_f32 v214, s51, v94\n"
    "v_mul_f32 v215, s51, v95\n"
    "v_exp_f32 v212, v212\n"
    "s_waitcnt vmcnt(28) lgkmcnt(4)\n"
    "ds_read_b64_tr_b16 v[200:201], v229 offset:40960\n"
    "ds_read_b64_tr_b16 v[202:203], v229 offset:41472\n"
    "v_mfma_f32_32x32x16_bf16 v[96:111], v[184:187], a[184:187], v[96:111]\n"
    "buffer_load_dwordx4 v225, s[28:31], s49 offen offset:1024 lds\n"
    "v_exp_f32 v213, v213\n"
    "v_exp_f32 v214, v214\n"
    "v_mfma_f32_32x32x16_bf16 v[112:127], v[184:187], a[188:191], v[112:127]\n"
    "v_exp_f32 v215, v215\n"
    "v_add_f32 v212, 1.0, v212\n"
    "v_add_f32 v213, 1.0, v213\n"
    "v_add_f32 v214, 1.0, v214\n"
    "v_add_f32 v215, 1.0, v215\n"
    "ds_read_b64_tr_b16 v[204:205], v246 offset:40960\n"
    "ds_read_b64_tr_b16 v[206:207], v246 offset:41472\n"
    "v_mfma_f32_32x32x16_bf16 v[128:143], v[188:191], a[184:187], v[128:143]\n"
    "s_add_u32 m0, s48, 0x4000\n"
    "s_add_u32 s49, s63, 0x48000\n"
    "buffer_load_dwordx4 v224, s[28:31], s49 offen lds\n"
    "v_rcp_f32 v212, v212\n"
    "v_rcp_f32 v213, v213\n"
    "v_mfma_f32_32x32x16_bf16 v[144:159], v[188:191], a[188:191], v[144:159]\n"
    "v_rcp_f32 v214, v214\n"
    "v_rcp_f32 v215, v215\n"
    "v_mul_f32 v212, v92, v212\n"
    "v_mul_f32 v213, v93, v213\n"
    "s_waitcnt vmcnt(28) lgkmcnt(4)\n"
    "ds_read_b64_tr_b16 v[176:177], v231 offset:0\n"
    "ds_read_b64_tr_b16 v[178:179], v231 offset:512\n"
    "v_mfma_f32_32x32x16_bf16 v[96:111], v[192:195], a[192:195], v[96:111]\n"
    "buffer_load_dwordx4 v225, s[28:31], s49 offen offset:1024 lds\n"
    "v_mul_f32 v214, v94, v214\n"
    "v_mul_f32 v215, v95, v215\n"
    "v_cvt_pk_bf16_f32 v86, v212, v213\n"
    "v_cvt_pk_bf16_f32 v87, v214, v215\n"
    "ds_write_b128 v237, v[80:83] offset:0\n"
    "ds_write_b128 v237, v[84:87] offset:1024\n"
    "v_mfma_f32_32x32x16_bf16 v[112:127], v[192:195], a[196:199], v[112:127]\n"
    "v_mul_f32 v208, s51, v160\n"
    "v_mul_f32 v209, s51, v161\n"
    "v_mul_f32 v210, s51, v162\n"
    "v_mul_f32 v211, s51, v163\n"
    "v_exp_f32 v208, v208\n"
    "ds_read_b64_tr_b16 v[180:181], v247 offset:0\n"
    "ds_read_b64_tr_b16 v[182:183], v247 offset:512\n"
    "v_mfma_f32_32x32x16_bf16 v[128:143], v[196:199], a[192:195], v[128:143]\n"
    "s_add_u32 m0, s48, 0x6000\n"
    "s_add_u32 s49, s63, 0x4c800\n"
    "buffer_load_dwordx4 v224, s[28:31], s49 offen lds\n"
    "v_exp_f32 v209, v209\n"
    "v_exp_f32 v210, v210\n"
    "v_mfma_f32_32x32x16_bf16 v[144:159], v[196:199], a[196:199], v[144:159]\n"
    "v_exp_f32 v211, v211\n"
    "v_add_f32 v208, 1.0, v208\n"
    "v_add_f32 v209, 1.0, v209\n"
    "v_add_f32 v210, 1.0, v210\n"
    "v_add_f32 v211, 1.0, v211\n"
    "s_waitcnt vmcnt(28) lgkmcnt(6)\n"
    "ds_read_b64_tr_b16 v[184:185], v228 offset:32768\n"
    "ds_read_b64_tr_b16 v[186:187], v228 offset:33280\n"
    "v_mfma_f32_32x32x16_bf16 v[96:111], v[200:203], a[200:203], v[96:111]\n"
    "buffer_load_dwordx4 v225, s[28:31], s49 offen offset:1024 lds\n"
    "v_rcp_f32 v208, v208\n"
    "v_rcp_f32 v209, v209\n"
    "s_waitcnt lgkmcnt(4)\n"
    "s_barrier\n"
    "v_mfma_f32_32x32x16_bf16 v[112:127], v[200:203], a[204:207], v[112:127]\n"
    "v_rcp_f32 v210, v210\n"
    "v_rcp_f32 v211, v211\n"
    "v_mul_f32 v208, v160, v208\n"
    "v_mul_f32 v209, v161, v209\n"
    "ds_read_b128 a[64:67], v234 offset:0\n"
    "ds_read_b128 a[68:71], v235 offset:0\n"
    "ds_read_b64_tr_b16 v[188:189], v232 offset:32768\n"
    "ds_read_b64_tr_b16 v[190:191], v232 offset:33280\n"
    "v_mfma_f32_32x32x16_bf16 v[128:143], v[204:207], a[200:203], v[128:143]\n"
    "s_add_u32 m0, s48, 0x18000\n"
    "s_add_u32 s49, s63, 0x6c000\n"
    "buffer_load_dwordx4 v224, s[28:31], s49 offen lds\n"
    "v_mul_f32 v210, v162, v210\n"
    "v_mul_f32 v211, v163, v211\n"
    "v_cvt_pk_bf16_f32 v160, v208, v209\n"
    "v_cvt_pk_bf16_f32 v161, v210, v211\n"
    "ds_read_b128 a[72:75], v234 offset:1024\n"
    "ds_read_b128 a[76:79], v235 offset:1024\n"
    "v_mfma_f32_32x32x16_bf16 v[144:159], v[204:207], a[204:207], v[144:159]\n"
    "v_mul_f32 v212, s51, v164\n"
    "v_mul_f32 v213, s51, v165\n"
    "v_mul_f32 v214, s51, v166\n"
    "v_mul_f32 v215, s51, v167\n"
    "v_exp_f32 v212, v212\n"
    "ds_read_b128 a[80:83], v234 offset:2048\n"
    "ds_read_b128 a[84:87], v235 offset:2048\n"
    "s_waitcnt vmcnt(28) lgkmcnt(10)\n"
    "ds_read_b64_tr_b16 v[192:193], v228 offset:57344\n"
    "ds_read_b64_tr_b16 v[194:195], v228 offset:57856\n"
    "v_mfma_f32_32x32x16_bf16 v[96:111], v[176:179], a[208:211], v[96:111]\n"
    "buffer_load_dwordx4 v225, s[28:31], s49 offen offset:1024 lds\n"
    "v_exp_f32 v213, v213\n"
    "v_exp_f32 v214, v214\n"
    "ds_read_b128 a[88:91], v234 offset:3072\n"
    "ds_read_b128 a[92:95], v235 offset:3072\n"
    "v_mfma_f32_32x32x16_bf16 v[112:127], v[176:179], a[212:215], v[112:127]\n"
    "v_exp_f32 v215, v215\n"
    "v_add_f32 v212, 1.0, v212\n"
    "v_add_f32 v213, 1.0, v213\n"
    "v_add_f32 v214, 1.0, v214\n"
    "v_add_f32 v215, 1.0, v215\n"
    "ds_read_b128 a[96:99], v234 offset:4096\n"
    "ds_read_b128 a[100:103], v235 offset:4096\n"
    "ds_read_b64_tr_b16 v[196:197], v232 offset:57344\n"
    "ds_read_b64_tr_b16 v[198:199], v232 offset:57856\n"
    "v_mfma_f32_32x32x16_bf16 v[128:143], v[180:183], a[208:211], v[128:143]\n"
    "s_add_u32 m0, s48, 0x1a000\n"
    "s_add_u32 s49, s63, 0x70800\n"
    "buffer_load_dwordx4 v224, s[28:31], s49 offen lds\n"
    "v_rcp_f32 v212, v212\n"
    "v_rcp_f32 v213, v213\n"
    "ds_read_b128 a[104:107], v234 offset:5120\n"
    "ds_read_b128 a[108:111], v235 offset:5120\n"
    "v_mfma_f32_32x32x16_bf16 v[144:159], v[180:183], a[212:215], v[144:159]\n"
    "v_rcp_f32 v214, v214\n"
    "v_rcp_f32 v215, v215\n"
    "v_mul_f32 v212, v164, v212\n"
    "v_mul_f32 v213, v165, v213\n"
    "ds_read_b128 a[112:115], v234 offset:6144\n"
    "ds_read_b128 a[116:119], v235 offset:6144\n"
    "s_waitcnt vmcnt(27) lgkmcnt(13)\n"
    "ds_read_b64_tr_b16 v[200:201], v229 offset:16384\n"
    "ds_read_b64_tr_b16 v[202:203], v229 offset:16896\n"
    "v_mfma_f32_32x32x16_bf16 v[96:111], v[184:187], a[216:219], v[96:111]\n"
    "buffer_load_dwordx4 v225, s[28:31], s49 offen offset:1024 lds\n"
    "v_mul_f32 v214, v166, v214\n"
    "v_mul_f32 v215, v167, v215\n"
    "v_cvt_pk_bf16_f32 v162, v212, v213\n"
    "v_cvt_pk_bf16_f32 v163, v214, v215\n"
    "ds_read_b128 a[120:123], v234 offset:7168\n"
    "ds_read_b128 a[124:127], v235 offset:7168\n"
    "v_mfma_f32_32x32x16_bf16 v[112:127], v[184:187], a[220:223], v[112:127]\n"
    "v_mul_f32 v208, s51, v168\n"
    "v_mul_f32 v209, s51, v169\n"
    "v_mul_f32 v210, s51, v170\n"
    "v_mul_f32 v211, s51, v171\n"
    "v_exp_f32 v208, v208\n"
    "ds_read_b64_tr_b16 v[204:205], v246 offset:16384\n"
    "ds_read_b64_tr_b16 v[206:207], v246 offset:16896\n"
    "v_mfma_f32_32x32x16_bf16 v[128:143], v[188:191], a[216:219], v[128:143]\n"
    "s_add_u32 m0, s48, 0x20000\n"
    "s_add_u32 s49, s63, 0x9000\n"
    "buffer_load_dwordx4 v224, s[28:31], s49 offen lds\n"
    "v_exp_f32 v209, v209\n"
    "v_exp_f32 v210, v210\n"
    "v_mfma_f32_32x32x16_bf16 v[144:159], v[188:191], a[220:223], v[144:159]\n"
    "v_exp_f32 v211, v211\n"
    "v_add_f32 v208, 1.0, v208\n"
    "v_add_f32 v209, 1.0, v209\n"
    "v_add_f32 v210, 1.0, v210\n"
    "v_add_f32 v211, 1.0, v211\n"
    "s_waitcnt vmcnt(27) lgkmcnt(10)\n"
    "ds_read_b64_tr_b16 v[176:177], v229 offset:49152\n"
    "ds_read_b64_tr_b16 v[178:179], v229 offset:49664\n"
    "v_mfma_f32_32x32x16_bf16 v[96:111], v[192:195], a[224:227], v[96:111]\n"
    "buffer_load_dwordx4 v225, s[28:31], s49 offen offset:1024 lds\n"
    "v_rcp_f32 v208, v208\n"
    "v_rcp_f32 v209, v209\n"
    "v_mfma_f32_32x32x16_bf16 v[112:127], v[192:195], a[228:231], v[112:127]\n"
    "v_rcp_f32 v210, v210\n"
    "v_rcp_f32 v211, v211\n"
    "v_mul_f32 v208, v168, v208\n"
    "v_mul_f32 v209, v169, v209\n"
    "ds_read_b64_tr_b16 v[180:181], v246 offset:49152\n"
    "ds_read_b64_tr_b16 v[182:183], v246 offset:49664\n"
    "v_mfma_f32_32x32x16_bf16 v[128:143], v[196:199], a[224:227], v[128:143]\n"
    "s_add_u32 m0, s48, 0x8000\n"
    "s_add_u32 s49, s63, 0xd800\n"
    "buffer_load_dwordx4 v224, s[28:31], s49 offen lds\n"
    "v_mul_f32 v210, v170, v210\n"
    "v_mul_f32 v211, v171, v211\n"
    "v_cvt_pk_bf16_f32 v164, v208, v209\n"
    "v_cvt_pk_bf16_f32 v165, v210, v211\n"
    "s_waitcnt lgkmcnt(6)\n"
    "s_barrier\n"
    "v_mfma_f32_32x32x16_bf16 v[144:159], v[196:199], a[228:231], v[144:159]\n"
    "v_mul_f32 v212, s51, v172\n"
    "v_mul_f32 v213, s51, v173\n"
    "v_mul_f32 v214, s51, v174\n"
    "v_mul_f32 v215, s51, v175\n"
    "v_exp_f32 v212, v212\n"
    "s_waitcnt vmcnt(27) lgkmcnt(4)\n"
    "ds_read_b64_tr_b16 v[184:185], v231 offset:8192\n"
    "ds_read_b64_tr_b16 v[186:187], v231 offset:8704\n"
    "v_mfma_f32_32x32x16_bf16 v[96:111], v[200:203], a[232:235], v[96:111]\n"
    "buffer_load_dwordx4 v225, s[28:31], s49 offen offset:1024 lds\n"
    "v_exp_f32 v213, v213\n"
    "v_exp_f32 v214, v214\n"
    "v_mfma_f32_32x32x16_bf16 v[112:127], v[200:203], a[236:239], v[112:127]\n"
    "v_exp_f32 v215, v215\n"
    "v_add_f32 v212, 1.0, v212\n"
    "v_add_f32 v213, 1.0, v213\n"
    "v_add_f32 v214, 1.0, v214\n"
    "v_add_f32 v215, 1.0, v215\n"
    "ds_read_b64_tr_b16 v[188:189], v247 offset:8192\n"
    "ds_read_b64_tr_b16 v[190:191], v247 offset:8704\n"
    "v_mfma_f32_32x32x16_bf16 v[128:143], v[204:207], a[232:235], v[128:143]\n"
    "s_add_u32 m0, s48, 0xe000\n"
    "s_add_u32 s49, s63, 0x2d000\n"
    "buffer_load_dwordx4 v224, s[28:31], s49 offen lds\n"
    "v_rcp_f32 v212, v212\n"
    "v_rcp_f32 v213, v213\n"
    "v_mfma_f32_32x32x16_bf16 v[144:159], v[204:207], a[236:239], v[144:159]\n"
    "v_rcp_f32 v214, v214\n"
    "v_rcp_f32 v215, v215\n"
    "v_mul_f32 v212, v172, v212\n"
    "v_mul_f32 v213, v173, v213\n"
    "s_waitcnt vmcnt(27) lgkmcnt(4)\n"
    "ds_read_b64_tr_b16 v[192:193], v228 offset:0\n"
    "ds_read_b64_tr_b16 v[194:195], v228 offset:512\n"
    "v_mfma_f32_32x32x16_bf16 v[96:111], v[176:179], a[240:243], v[96:111]\n"
    "buffer_load_dwordx4 v225, s[28:31], s49 offen offset:1024 lds\n"
    "v_mul_f32 v214, v174, v214\n"
    "v_mul_f32 v215, v175, v215\n"
    "v_cvt_pk_bf16_f32 v166, v212, v213\n"
    "v_cvt_pk_bf16_f32 v167, v214, v215\n"
    "ds_write_b128 v238, v[160:163] offset:0\n"
    "ds_write_b128 v238, v[164:167] offset:1024\n"
    "v_mfma_f32_32x32x16_bf16 v[112:127], v[176:179], a[244:247], v[112:127]\n"
    "ds_read_b64_tr_b16 v[196:197], v232 offset:0\n"
    "ds_read_b64_tr_b16 v[198:199], v232 offset:512\n"
    "v_mfma_f32_32x32x16_bf16 v[128:143], v[180:183], a[240:243], v[128:143]\n"
    "s_add_u32 m0, s48, 0x14000\n"
    "s_add_u32 s49, s63, 0x31800\n"
    "buffer_load_dwordx4 v224, s[28:31], s49 offen lds\n"
    "v_mfma_f32_32x32x16_bf16 v[144:159], v[180:183], a[244:247], v[144:159]\n"
    "s_waitcnt vmcnt(27) lgkmcnt(6)\n"
    "ds_read_b64_tr_b16 v[200:201], v229 offset:0\n"
    "ds_read_b64_tr_b16 v[202:203], v229 offset:512\n"
    "v_mfma_f32_32x32x16_bf16 v[96:111], v[184:187], a[248:251], v[96:111]\n"
    "buffer_load_dwordx4 v225, s[28:31], s49 offen offset:1024 lds\n"
    "s_waitcnt lgkmcnt(4)\n"
    "s_barrier\n"
    "v_mfma_f32_32x32x16_bf16 v[112:127], v[184:187], a[252:255], v[112:127]\n"
    "ds_read_b128 a[128:131], v234 offset:0\n"
    "ds_read_b128 a[132:135], v235 offset:0\n"
    "ds_read_b64_tr_b16 v[204:205], v246 offset:0\n"
    "ds_read_b64_tr_b16 v[206:207], v246 offset:512\n"
    "v_mfma_f32_32x32x16_bf16 v[128:143], v[188:191], a[248:251], v[128:143]\n"
    "s_add_u32 m0, s48, 0x1c000\n"
    "s_add_u32 s49, s63, 0x51000\n"
    "buffer_load_dwordx4 v224, s[28:31], s49 offen lds\n"
    "ds_read_b128 a[136:139], v234 offset:1024\n"
    "ds_read_b128 a[140:143], v235 offset:1024\n"
    "v_mfma_f32_32x32x16_bf16 v[144:159], v[188:191], a[252:255], v[144:159]\n"
    "ds_read_b128 a[144:147], v234 offset:2048\n"
    "ds_read_b128 a[148:151], v235 offset:2048\n"
    "s_waitcnt vmcnt(27) lgkmcnt(10)\n"
    "ds_read_b64_tr_b16 v[176:177], v229 offset:24576\n"
    "ds_read_b64_tr_b16 v[178:179], v229 offset:25088\n"
    "v_mfma_f32_32x32x16_bf16 v[96:111], v[192:195], v[0:3], v[96:111]\n"
    "buffer_load_dwordx4 v225, s[28:31], s49 offen offset:1024 lds\n"
    "ds_read_b128 a[152:155], v234 offset:3072\n"
    "ds_read_b128 a[156:159], v235 offset:3072\n"
    "v_mfma_f32_32x32x16_bf16 v[112:127], v[192:195], v[4:7], v[112:127]\n"
    "ds_read_b64_tr_b16 v[180:181], v246 offset:24576\n"
    "ds_read_b64_tr_b16 v[182:183], v246 offset:25088\n"
    "v_mfma_f32_32x32x16_bf16 v[128:143], v[196:199], v[0:3], v[128:143]\n"
    "s_add_u32 m0, s48, 0x22000\n"
    "s_add_u32 s49, s63, 0x55800\n"
    "buffer_load_dwordx4 v224, s[28:31], s49 offen lds\n"
    "v_mfma_f32_32x32x16_bf16 v[144:159], v[196:199], v[4:7], v[144:159]\n"
    "s_waitcnt vmcnt(27) lgkmcnt(10)\n"
    "ds_read_b64_tr_b16 v[184:185], v228 offset:8192\n"
    "ds_read_b64_tr_b16 v[186:187], v228 offset:8704\n"
    "v_mfma_f32_32x32x16_bf16 v[96:111], v[200:203], v[8:11], v[96:111]\n"
    "buffer_load_dwordx4 v225, s[28:31], s49 offen offset:1024 lds\n"
    "v_mfma_f32_32x32x16_bf16 v[112:127], v[200:203], v[12:15], v[112:127]\n"
    "ds_read_b64_tr_b16 v[188:189], v232 offset:8192\n"
    "ds_read_b64_tr_b16 v[190:191], v232 offset:8704\n"
    "v_mfma_f32_32x32x16_bf16 v[128:143], v[204:207], v[8:11], v[128:143]\n"
    "s_add_u32 m0, s48, 0\n"
    "s_add_u32 s49, s63, 0x75000\n"
    "buffer_load_dwordx4 v224, s[28:31], s49 offen lds\n"
    "s_waitcnt lgkmcnt(6)\n"
    "s_barrier\n"
    "v_mfma_f32_32x32x16_bf16 v[144:159], v[204:207], v[12:15], v[144:159]\n"
    "s_waitcnt vmcnt(27)\n"
    "v_sub_u32 v228, v228, v248\n"
    "v_sub_u32 v229, v229, v248\n"
    "v_sub_u32 v231, v231, v248\n"
    "v_sub_u32 v232, v232, v248\n"
    "v_sub_u32 v246, v246, v248\n"
    "v_sub_u32 v247, v247, v248\n"
    "ds_read_b64_tr_b16 v[192:193], v228 offset:40960\n"
    "ds_read_b64_tr_b16 v[194:195], v228 offset:41984\n"
    "s_waitcnt lgkmcnt(6)\n"
    "v_mfma_f32_32x32x16_bf16 v[96:111], v[176:179], v[16:19], v[96:111]\n"
    "buffer_load_dwordx4 v225, s[28:31], s49 offen offset:1024 lds\n"
    "v_mfma_f32_32x32x16_bf16 v[112:127], v[176:179], v[20:23], v[112:127]\n"
    "ds_read_b64_tr_b16 v[196:197], v232 offset:40960\n"
    "ds_read_b64_tr_b16 v[198:199], v232 offset:41984\n"
    "v_mfma_f32_32x32x16_bf16 v[128:143], v[180:183], v[16:19], v[128:143]\n"
    "s_add_u32 m0, s48, 0x10000\n"
    "s_add_u32 s49, s63, 0x79800\n"
    "buffer_load_dwordx4 v224, s[28:31], s49 offen lds\n"
    "v_mfma_f32_32x32x16_bf16 v[144:159], v[180:183], v[20:23], v[144:159]\n"
    "s_waitcnt vmcnt(27) lgkmcnt(4)\n"
    "ds_read_b64_tr_b16 v[200:201], v228 offset:49152\n"
    "ds_read_b64_tr_b16 v[202:203], v228 offset:50176\n"
    "v_mfma_f32_32x32x16_bf16 v[96:111], v[184:187], v[24:27], v[96:111]\n"
    "buffer_load_dwordx4 v225, s[28:31], s49 offen offset:1024 lds\n"
    "v_mfma_f32_32x32x16_bf16 v[112:127], v[184:187], v[28:31], v[112:127]\n"
    "ds_read_b64_tr_b16 v[204:205], v232 offset:49152\n"
    "ds_read_b64_tr_b16 v[206:207], v232 offset:50176\n"
    "v_mfma_f32_32x32x16_bf16 v[128:143], v[188:191], v[24:27], v[128:143]\n"
    "s_add_u32 m0, s48, 0x16000\n"
    "s_add_u32 s49, s63, 0x90000\n"
    "buffer_load_dwordx4 v224, s[28:31], s49 offen lds\n"
    "v_mfma_f32_32x32x16_bf16 v[144:159], v[188:191], v[28:31], v[144:159]\n"
    "s_waitcnt vmcnt(27) lgkmcnt(4)\n"
    "ds_read_b64_tr_b16 v[176:177], v229 offset:8192\n"
    "ds_read_b64_tr_b16 v[178:179], v229 offset:9216\n"
    "v_mfma_f32_32x32x16_bf16 v[32:47], v[192:195], a[0:3], 0\n"
    "buffer_load_dwordx4 v225, s[28:31], s49 offen offset:1024 lds\n"
    "v_mfma_f32_32x32x16_bf16 v[48:63], v[192:195], a[4:7], 0\n"
    "v_mul_f32 v208, s51, v96\n"
    "v_mul_f32 v209, s51, v97\n"
    "v_mul_f32 v210, s51, v98\n"
    "v_mul_f32 v211, s51, v99\n"
    "v_exp_f32 v208, v208\n"
    "ds_read_b64_tr_b16 v[180:181], v246 offset:8192\n"
    "ds_read_b64_tr_b16 v[182:183], v246 offset:9216\n"
    "v_mfma_f32_32x32x16_bf16 v[64:79], v[196:199], a[0:3], 0\n"
    "s_add_u32 m0, s48, 0x2000\n"
    "s_add_u32 s49, s63, 0x94800\n"
    "buffer_load_dwordx4 v224, s[28:31], s49 offen lds\n"
    "v_exp_f32 v209, v209\n"
    "v_exp_f32 v210, v210\n"
    "v_mfma_f32_32x32x16_bf16 v[80:95], v[196:199], a[4:7], 0\n"
    "v_exp_f32 v211, v211\n"
    "v_add_f32 v208, 1.0, v208\n"
    "v_add_f32 v209, 1.0, v209\n"
    "v_add_f32 v210, 1.0, v210\n"
    "v_add_f32 v211, 1.0, v211\n"
    "s_waitcnt vmcnt(27) lgkmcnt(4)\n"
    "ds_read_b64_tr_b16 v[184:185], v229 offset:57344\n"
    "ds_read_b64_tr_b16 v[186:187], v229 offset:58368\n"
    "v_mfma_f32_32x32x16_bf16 v[32:47], v[200:203], a[8:11], v[32:47]\n"
    "buffer_load_dwordx4 v225, s[28:31], s49 offen offset:1024 lds\n"
    "v_rcp_f32 v208, v208\n"
    "v_rcp_f32 v209, v209\n"
    "v_mfma_f32_32x32x16_bf16 v[48:63], v[200:203], a[12:15], v[48:63]\n"
    "v_rcp_f32 v210, v210\n"
    "v_rcp_f32 v211, v211\n"
    "v_mul_f32 v208, v96, v208\n"
    "v_mul_f32 v209, v97, v209\n"
    "ds_read_b64_tr_b16 v[188:189], v246 offset:57344\n"
    "ds_read_b64_tr_b16 v[190:191], v246 offset:58368\n"
    "v_mfma_f32_32x32x16_bf16 v[64:79], v[204:207], a[8:11], v[64:79]\n"
    "s_add_u32 m0, s48, 0xa000\n"
    "s_add_u32 s49, s63, 0x99000\n"
    "buffer_load_dwordx4 v224, s[28:31], s49 offen lds\n"
    "v_mul_f32 v210, v98, v210\n"
    "v_mul_f32 v211, v99, v211\n"
    "v_cvt_pk_bf16_f32 v96, v208, v209\n"
    "v_cvt_pk_bf16_f32 v97, v210, v211\n"
    "v_mfma_f32_32x32x16_bf16 v[80:95], v[204:207], a[12:15], v[80:95]\n"
    "v_mul_f32 v212, s51, v100\n"
    "v_mul_f32 v213, s51, v101\n"
    "v_mul_f32 v214, s51, v102\n"
    "v_mul_f32 v215, s51, v103\n"
    "v_exp_f32 v212, v212\n"
    "s_waitcnt vmcnt(27) lgkmcnt(4)\n"
    "ds_read_b64_tr_b16 v[192:193], v228 offset:16384\n"
    "ds_read_b64_tr_b16 v[194:195], v228 offset:17408\n"
    "v_mfma_f32_32x32x16_bf16 v[32:47], v[176:179], a[16:19], v[32:47]\n"
    "buffer_load_dwordx4 v225, s[28:31], s49 offen offset:1024 lds\n"
    "v_exp_f32 v213, v213\n"
    "v_exp_f32 v214, v214\n"
    "v_mfma_f32_32x32x16_bf16 v[48:63], v[176:179], a[20:23], v[48:63]\n"
    "v_exp_f32 v215, v215\n"
    "v_add_f32 v212, 1.0, v212\n"
    "v_add_f32 v213, 1.0, v213\n"
    "v_add_f32 v214, 1.0, v214\n"
    "v_add_f32 v215, 1.0, v215\n"
    "ds_read_b64_tr_b16 v[196:197], v232 offset:16384\n"
    "ds_read_b64_tr_b16 v[198:199], v232 offset:17408\n"
    "v_mfma_f32_32x32x16_bf16 v[64:79], v[180:183], a[16:19], v[64:79]\n"
    "s_add_u32 m0, s48, 0xc000\n"
    "s_add_u32 s49, s63, 0x9d800\n"
    "buffer_load_dwordx4 v224, s[28:31], s49 offen lds\n"
    "v_rcp_f32 v212, v212\n"
    "v_rcp_f32 v213, v213\n"
    "v_mfma_f32_32x32x16_bf16 v[80:95], v[180:183], a[20:23], v[80:95]\n"
    "v_rcp_f32 v214, v214\n"
    "v_rcp_f32 v215, v215\n"
    "v_mul_f32 v212, v100, v212\n"
    "v_mul_f32 v213, v101, v213\n"
    "s_waitcnt vmcnt(27) lgkmcnt(4)\n"
    "ds_read_b64_tr_b16 v[200:201], v228 offset:24576\n"
    "ds_read_b64_tr_b16 v[202:203], v228 offset:25600\n"
    "v_mfma_f32_32x32x16_bf16 v[32:47], v[184:187], a[24:27], v[32:47]\n"
    "buffer_load_dwordx4 v225, s[28:31], s49 offen offset:1024 lds\n"
    "v_mul_f32 v214, v102, v214\n"
    "v_mul_f32 v215, v103, v215\n"
    "v_cvt_pk_bf16_f32 v98, v212, v213\n"
    "v_cvt_pk_bf16_f32 v99, v214, v215\n"
    "v_mfma_f32_32x32x16_bf16 v[48:63], v[184:187], a[28:31], v[48:63]\n"
    "v_mul_f32 v208, s51, v104\n"
    "v_mul_f32 v209, s51, v105\n"
    "v_mul_f32 v210, s51, v106\n"
    "v_mul_f32 v211, s51, v107\n"
    "v_exp_f32 v208, v208\n"
    "ds_read_b64_tr_b16 v[204:205], v232 offset:24576\n"
    "ds_read_b64_tr_b16 v[206:207], v232 offset:25600\n"
    "v_mfma_f32_32x32x16_bf16 v[64:79], v[188:191], a[24:27], v[64:79]\n"
    "s_add_u32 m0, s48, 0x12000\n"
    "s_add_u32 s49, s63, 0x12000\n"
    "buffer_load_dwordx4 v224, s[28:31], s49 offen lds\n"
    "v_exp_f32 v209, v209\n"
    "v_exp_f32 v210, v210\n"
    "v_mfma_f32_32x32x16_bf16 v[80:95], v[188:191], a[28:31], v[80:95]\n"
    "v_exp_f32 v211, v211\n"
    "v_add_f32 v208, 1.0, v208\n"
    "v_add_f32 v209, 1.0, v209\n"
    "v_add_f32 v210, 1.0, v210\n"
    "v_add_f32 v211, 1.0, v211\n"
    "s_waitcnt vmcnt(27) lgkmcnt(4)\n"
    "ds_read_b64_tr_b16 v[176:177], v229 offset:32768\n"
    "ds_read_b64_tr_b16 v[178:179], v229 offset:33792\n"
    "v_mfma_f32_32x32x16_bf16 v[32:47], v[192:195], a[32:35], v[32:47]\n"
    "buffer_load_dwordx4 v225, s[28:31], s49 offen offset:1024 lds\n"
    "v_rcp_f32 v208, v208\n"
    "v_rcp_f32 v209, v209\n"
    "v_mfma_f32_32x32x16_bf16 v[48:63], v[192:195], a[36:39], v[48:63]\n"
    "v_rcp_f32 v210, v210\n"
    "v_rcp_f32 v211, v211\n"
    "v_mul_f32 v208, v104, v208\n"
    "v_mul_f32 v209, v105, v209\n"
    "ds_read_b64_tr_b16 v[180:181], v246 offset:32768\n"
    "ds_read_b64_tr_b16 v[182:183], v246 offset:33792\n"
    "v_mfma_f32_32x32x16_bf16 v[64:79], v[196:199], a[32:35], v[64:79]\n"
    "s_add_u32 m0, s48, 0x1e000\n"
    "s_add_u32 s49, s63, 0x16800\n"
    "buffer_load_dwordx4 v224, s[28:31], s49 offen lds\n"
    "v_mul_f32 v210, v106, v210\n"
    "v_mul_f32 v211, v107, v211\n"
    "v_cvt_pk_bf16_f32 v100, v208, v209\n"
    "v_cvt_pk_bf16_f32 v101, v210, v211\n"
    "v_mfma_f32_32x32x16_bf16 v[80:95], v[196:199], a[36:39], v[80:95]\n"
    "v_mul_f32 v212, s51, v108\n"
    "v_mul_f32 v213, s51, v109\n"
    "v_mul_f32 v214, s51, v110\n"
    "v_mul_f32 v215, s51, v111\n"
    "v_exp_f32 v212, v212\n"
    "s_waitcnt vmcnt(27) lgkmcnt(4)\n"
    "ds_read_b64_tr_b16 v[184:185], v229 offset:40960\n"
    "ds_read_b64_tr_b16 v[186:187], v229 offset:41984\n"
    "v_mfma_f32_32x32x16_bf16 v[32:47], v[200:203], a[40:43], v[32:47]\n"
    "buffer_load_dwordx4 v225, s[28:31], s49 offen offset:1024 lds\n"
    "v_exp_f32 v213, v213\n"
    "v_exp_f32 v214, v214\n"
    "v_mfma_f32_32x32x16_bf16 v[48:63], v[200:203], a[44:47], v[48:63]\n"
    "v_exp_f32 v215, v215\n"
    "v_add_f32 v212, 1.0, v212\n"
    "v_add_f32 v213, 1.0, v213\n"
    "v_add_f32 v214, 1.0, v214\n"
    "v_add_f32 v215, 1.0, v215\n"
    "ds_read_b64_tr_b16 v[188:189], v246 offset:40960\n"
    "ds_read_b64_tr_b16 v[190:191], v246 offset:41984\n"
    "v_mfma_f32_32x32x16_bf16 v[64:79], v[204:207], a[40:43], v[64:79]\n"
    "s_add_u32 m0, s48, 0x4000\n"
    "s_add_u32 s49, s63, 0x36000\n"
    "buffer_load_dwordx4 v224, s[28:31], s49 offen lds\n"
    "v_rcp_f32 v212, v212\n"
    "v_rcp_f32 v213, v213\n"
    "v_mfma_f32_32x32x16_bf16 v[80:95], v[204:207], a[44:47], v[80:95]\n"
    "v_rcp_f32 v214, v214\n"
    "v_rcp_f32 v215, v215\n"
    "v_mul_f32 v212, v108, v212\n"
    "v_mul_f32 v213, v109, v213\n"
    "s_waitcnt vmcnt(27) lgkmcnt(4)\n"
    "ds_read_b64_tr_b16 v[192:193], v231 offset:0\n"
    "ds_read_b64_tr_b16 v[194:195], v231 offset:1024\n"
    "v_mfma_f32_32x32x16_bf16 v[32:47], v[176:179], a[48:51], v[32:47]\n"
    "buffer_load_dwordx4 v225, s[28:31], s49 offen offset:1024 lds\n"
    "v_mul_f32 v214, v110, v214\n"
    "v_mul_f32 v215, v111, v215\n"
    "v_cvt_pk_bf16_f32 v102, v212, v213\n"
    "v_cvt_pk_bf16_f32 v103, v214, v215\n"
    "ds_write_b128 v236, v[96:99] offset:0\n"
    "ds_write_b128 v236, v[100:103] offset:1024\n"
    "v_mfma_f32_32x32x16_bf16 v[48:63], v[176:179], a[52:55], v[48:63]\n"
    "v_mul_f32 v208, s51, v112\n"
    "v_mul_f32 v209, s51, v113\n"
    "v_mul_f32 v210, s51, v114\n"
    "v_mul_f32 v211, s51, v115\n"
    "v_exp_f32 v208, v208\n"
    "ds_read_b64_tr_b16 v[196:197], v247 offset:0\n"
    "ds_read_b64_tr_b16 v[198:199], v247 offset:1024\n"
    "v_mfma_f32_32x32x16_bf16 v[64:79], v[180:183], a[48:51], v[64:79]\n"
    "s_add_u32 m0, s48, 0x6000\n"
    "s_add_u32 s49, s63, 0x3a800\n"
    "buffer_load_dwordx4 v224, s[28:31], s49 offen lds\n"
    "v_exp_f32 v209, v209\n"
    "v_exp_f32 v210, v210\n"
    "v_mfma_f32_32x32x16_bf16 v[80:95], v[180:183], a[52:55], v[80:95]\n"
    "v_exp_f32 v211, v211\n"
    "v_add_f32 v208, 1.0, v208\n"
    "v_add_f32 v209, 1.0, v209\n"
    "v_add_f32 v210, 1.0, v210\n"
    "v_add_f32 v211, 1.0, v211\n"
    "s_waitcnt vmcnt(27) lgkmcnt(6)\n"
    "ds_read_b64_tr_b16 v[200:201], v228 offset:32768\n"
    "ds_read_b64_tr_b16 v[202:203], v228 offset:33792\n"
    "v_mfma_f32_32x32x16_bf16 v[32:47], v[184:187], a[56:59], v[32:47]\n"
    "buffer_load_dwordx4 v225, s[28:31], s49 offen offset:1024 lds\n"
    "v_rcp_f32 v208, v208\n"
    "v_rcp_f32 v209, v209\n"
    "v_mfma_f32_32x32x16_bf16 v[48:63], v[184:187], a[60:63], v[48:63]\n"
    "v_rcp_f32 v210, v210\n"
    "v_rcp_f32 v211, v211\n"
    "v_mul_f32 v208, v112, v208\n"
    "v_mul_f32 v209, v113, v209\n"
    "ds_read_b64_tr_b16 v[204:205], v232 offset:32768\n"
    "ds_read_b64_tr_b16 v[206:207], v232 offset:33792\n"
    "v_mfma_f32_32x32x16_bf16 v[64:79], v[188:191], a[56:59], v[64:79]\n"
    "s_add_u32 m0, s48, 0x18000\n"
    "s_add_u32 s49, s63, 0x5a000\n"
    "buffer_load_dwordx4 v224, s[28:31], s49 offen lds\n"
    "v_mul_f32 v210, v114, v210\n"
    "v_mul_f32 v211, v115, v211\n"
    "v_cvt_pk_bf16_f32 v112, v208, v209\n"
    "v_cvt_pk_bf16_f32 v113, v210, v211\n"
    "v_mfma_f32_32x32x16_bf16 v[80:95], v[188:191], a[60:63], v[80:95]\n"
    "v_mul_f32 v212, s51, v116\n"
    "v_mul_f32 v213, s51, v117\n"
    "v_mul_f32 v214, s51, v118\n"
    "v_mul_f32 v215, s51, v119\n"
    "v_exp_f32 v212, v212\n"
    "s_waitcnt vmcnt(27) lgkmcnt(4)\n"
    "ds_read_b64_tr_b16 v[176:177], v228 offset:57344\n"
    "ds_read_b64_tr_b16 v[178:179], v228 offset:58368\n"
    "v_mfma_f32_32x32x16_bf16 v[32:47], v[192:195], a[64:67], v[32:47]\n"
    "buffer_load_dwordx4 v225, s[28:31], s49 offen offset:1024 lds\n"
    "v_exp_f32 v213, v213\n"
    "v_exp_f32 v214, v214\n"
    "v_mfma_f32_32x32x16_bf16 v[48:63], v[192:195], a[68:71], v[48:63]\n"
    "v_exp_f32 v215, v215\n"
    "v_add_f32 v212, 1.0, v212\n"
    "v_add_f32 v213, 1.0, v213\n"
    "v_add_f32 v214, 1.0, v214\n"
    "v_add_f32 v215, 1.0, v215\n"
    "ds_read_b64_tr_b16 v[180:181], v232 offset:57344\n"
    "ds_read_b64_tr_b16 v[182:183], v232 offset:58368\n"
    "v_mfma_f32_32x32x16_bf16 v[64:79], v[196:199], a[64:67], v[64:79]\n"
    "s_add_u32 m0, s48, 0x1a000\n"
    "s_add_u32 s49, s63, 0x5e800\n"
    "buffer_load_dwordx4 v224, s[28:31], s49 offen lds\n"
    "v_rcp_f32 v212, v212\n"
    "v_rcp_f32 v213, v213\n"
    "v_mfma_f32_32x32x16_bf16 v[80:95], v[196:199], a[68:71], v[80:95]\n"
    "v_rcp_f32 v214, v214\n"
    "v_rcp_f32 v215, v215\n"
    "v_mul_f32 v212, v116, v212\n"
    "v_mul_f32 v213, v117, v213\n"
    "s_waitcnt vmcnt(27) lgkmcnt(4)\n"
    "ds_read_b64_tr_b16 v[184:185], v229 offset:16384\n"
    "ds_read_b64_tr_b16 v[186:187], v229 offset:17408\n"
    "v_mfma_f32_32x32x16_bf16 v[32:47], v[200:203], a[72:75], v[32:47]\n"
    "buffer_load_dwordx4 v225, s[28:31], s49 offen offset:1024 lds\n"
    "v_mul_f32 v214, v118, v214\n"
    "v_mul_f32 v215, v119, v215\n"
    "v_cvt_pk_bf16_f32 v114, v212, v213\n"
    "v_cvt_pk_bf16_f32 v115, v214, v215\n"
    "v_mfma_f32_32x32x16_bf16 v[48:63], v[200:203], a[76:79], v[48:63]\n"
    "v_mul_f32 v208, s51, v120\n"
    "v_mul_f32 v209, s51, v121\n"
    "v_mul_f32 v210, s51, v122\n"
    "v_mul_f32 v211, s51, v123\n"
    "v_exp_f32 v208, v208\n"
    "ds_read_b64_tr_b16 v[188:189], v246 offset:16384\n"
    "ds_read_b64_tr_b16 v[190:191], v246 offset:17408\n"
    "v_mfma_f32_32x32x16_bf16 v[64:79], v[204:207], a[72:75], v[64:79]\n"
    "s_add_u32 m0, s48, 0x20000\n"
    "s_add_u32 s49, s63, 0x7e000\n"
    "buffer_load_dwordx4 v224, s[28:31], s49 offen lds\n"
    "v_exp_f32 v209, v209\n"
    "v_exp_f32 v210, v210\n"
    "v_mfma_f32_32x32x16_bf16 v[80:95], v[204:207], a[76:79], v[80:95]\n"
    "v_exp_f32 v211, v211\n"
    "v_add_f32 v208, 1.0, v208\n"
    "v_add_f32 v209, 1.0, v209\n"
    "v_add_f32 v210, 1.0, v210\n"
    "v_add_f32 v211, 1.0, v211\n"
    "s_waitcnt vmcnt(27) lgkmcnt(4)\n"
    "ds_read_b64_tr_b16 v[192:193], v229 offset:49152\n"
    "ds_read_b64_tr_b16 v[194:195], v229 offset:50176\n"
    "v_mfma_f32_32x32x16_bf16 v[32:47], v[176:179], a[80:83], v[32:47]\n"
    "buffer_load_dwordx4 v225, s[28:31], s49 offen offset:1024 lds\n"
    "v_rcp_f32 v208, v208\n"
    "v_rcp_f32 v209, v209\n"
    "v_mfma_f32_32x32x16_bf16 v[48:63], v[176:179], a[84:87], v[48:63]\n"
    "v_rcp_f32 v210, v210\n"
    "v_rcp_f32 v211, v211\n"
    "v_mul_f32 v208, v120, v208\n"
    "v_mul_f32 v209, v121, v209\n"
    "ds_read_b64_tr_b16 v[196:197], v246 offset:49152\n"
    "ds_read_b64_tr_b16 v[198:199], v246 offset:50176\n"
    "v_mfma_f32_32x32x16_bf16 v[64:79], v[180:183], a[80:83], v[64:79]\n"
    "s_add_u32 m0, s48, 0x8000\n"
    "s_add_u32 s49, s63, 0x82800\n"
    "buffer_load_dwordx4 v224, s[28:31], s49 offen lds\n"
    "v_mul_f32 v210, v122, v210\n"
    "v_mul_f32 v211, v123, v211\n"
    "v_cvt_pk_bf16_f32 v116, v208, v209\n"
    "v_cvt_pk_bf16_f32 v117, v210, v211\n"
    "v_mfma_f32_32x32x16_bf16 v[80:95], v[180:183], a[84:87], v[80:95]\n"
    "v_mul_f32 v212, s51, v124\n"
    "v_mul_f32 v213, s51, v125\n"
    "v_mul_f32 v214, s51, v126\n"
    "v_mul_f32 v215, s51, v127\n"
    "v_exp_f32 v212, v212\n"
    "s_waitcnt vmcnt(27) lgkmcnt(4)\n"
    "ds_read_b64_tr_b16 v[200:201], v231 offset:8192\n"
    "ds_read_b64_tr_b16 v[202:203], v231 offset:9216\n"
    "v_mfma_f32_32x32x16_bf16 v[32:47], v[184:187], a[88:91], v[32:47]\n"
    "buffer_load_dwordx4 v225, s[28:31], s49 offen offset:1024 lds\n"
    "v_exp_f32 v213, v213\n"
    "v_exp_f32 v214, v214\n"
    "v_mfma_f32_32x32x16_bf16 v[48:63], v[184:187], a[92:95], v[48:63]\n"
    "v_exp_f32 v215, v215\n"
    "v_add_f32 v212, 1.0, v212\n"
    "v_add_f32 v213, 1.0, v213\n"
    "v_add_f32 v214, 1.0, v214\n"
    "v_add_f32 v215, 1.0, v215\n"
    "ds_read_b64_tr_b16 v[204:205], v247 offset:8192\n"
    "ds_read_b64_tr_b16 v[206:207], v247 offset:9216\n"
    "v_mfma_f32_32x32x16_bf16 v[64:79], v[188:191], a[88:91], v[64:79]\n"
    "s_add_u32 m0, s48, 0xe000\n"
    "s_add_u32 s49, s63, 0x1b000\n"
    "buffer_load_dwordx4 v224, s[28:31], s49 offen lds\n"
    "v_rcp_f32 v212, v212\n"
    "v_rcp_f32 v213, v213\n"
    "v_mfma_f32_32x32x16_bf16 v[80:95], v[188:191], a[92:95], v[80:95]\n"
    "v_rcp_f32 v214, v214\n"
    "v_rcp_f32 v215, v215\n"
    "v_mul_f32 v212, v124, v212\n"
    "v_mul_f32 v213, v125, v213\n"
    "s_waitcnt vmcnt(27) lgkmcnt(4)\n"
    "ds_read_b64_tr_b16 v[176:177], v228 offset:0\n"
    "ds_read_b64_tr_b16 v[178:179], v228 offset:1024\n"
    "v_mfma_f32_32x32x16_bf16 v[32:47], v[192:195], a[96:99], v[32:47]\n"
    "buffer_load_dwordx4 v225, s[28:31], s49 offen offset:1024 lds\n"
    "v_mul_f32 v214, v126, v214\n"
    "v_mul_f32 v215, v127, v215\n"
    "v_cvt_pk_bf16_f32 v118, v212, v213\n"
    "v_cvt_pk_bf16_f32 v119, v214, v215\n"
    "ds_write_b128 v237, v[112:115] offset:0\n"
    "ds_write_b128 v237, v[116:119] offset:1024\n"
    "v_mfma_f32_32x32x16_bf16 v[48:63], v[192:195], a[100:103], v[48:63]\n"
    "v_mul_f32 v208, s51, v128\n"
    "v_mul_f32 v209, s51, v129\n"
    "v_mul_f32 v210, s51, v130\n"
    "v_mul_f32 v211, s51, v131\n"
    "v_exp_f32 v208, v208\n"
    "ds_read_b64_tr_b16 v[180:181], v232 offset:0\n"
    "ds_read_b64_tr_b16 v[182:183], v232 offset:1024\n"
    "v_mfma_f32_32x32x16_bf16 v[64:79], v[196:199], a[96:99], v[64:79]\n"
    "s_add_u32 m0, s48, 0x14000\n"
    "s_add_u32 s49, s63, 0x1f800\n"
    "buffer_load_dwordx4 v224, s[28:31], s49 offen lds\n"
    "v_exp_f32 v209, v209\n"
    "v_exp_f32 v210, v210\n"
    "v_mfma_f32_32x32x16_bf16 v[80:95], v[196:199], a[100:103], v[80:95]\n"
    "v_exp_f32 v211, v211\n"
    "v_add_f32 v208, 1.0, v208\n"
    "v_add_f32 v209, 1.0, v209\n"
    "v_add_f32 v210, 1.0, v210\n"
    "v_add_f32 v211, 1.0, v211\n"
    "s_waitcnt vmcnt(27) lgkmcnt(6)\n"
    "ds_read_b64_tr_b16 v[184:185], v229 offset:0\n"
    "ds_read_b64_tr_b16 v[186:187], v229 offset:1024\n"
    "v_mfma_f32_32x32x16_bf16 v[32:47], v[200:203], a[104:107], v[32:47]\n"
    "buffer_load_dwordx4 v225, s[28:31], s49 offen offset:1024 lds\n"
    "v_rcp_f32 v208, v208\n"
    "v_rcp_f32 v209, v209\n"
    "s_waitcnt lgkmcnt(4)\n"
    "s_barrier\n"
    "v_mfma_f32_32x32x16_bf16 v[48:63], v[200:203], a[108:111], v[48:63]\n"
    "v_rcp_f32 v210, v210\n"
    "v_rcp_f32 v211, v211\n"
    "v_mul_f32 v208, v128, v208\n"
    "v_mul_f32 v209, v129, v209\n"
    "ds_read_b128 a[160:163], v234 offset:0\n"
    "ds_read_b128 a[164:167], v235 offset:0\n"
    "ds_read_b64_tr_b16 v[188:189], v246 offset:0\n"
    "ds_read_b64_tr_b16 v[190:191], v246 offset:1024\n"
    "v_mfma_f32_32x32x16_bf16 v[64:79], v[204:207], a[104:107], v[64:79]\n"
    "s_add_u32 m0, s48, 0x1c000\n"
    "s_add_u32 s49, s63, 0x3f000\n"
    "buffer_load_dwordx4 v224, s[28:31], s49 offen lds\n"
    "v_mul_f32 v210, v130, v210\n"
    "v_mul_f32 v211, v131, v211\n"
    "v_cvt_pk_bf16_f32 v128, v208, v209\n"
    "v_cvt_pk_bf16_f32 v129, v210, v211\n"
    "ds_read_b128 a[168:171], v234 offset:1024\n"
    "ds_read_b128 a[172:175], v235 offset:1024\n"
    "v_mfma_f32_32x32x16_bf16 v[80:95], v[204:207], a[108:111], v[80:95]\n"
    "v_mul_f32 v212, s51, v132\n"
    "v_mul_f32 v213, s51, v133\n"
    "v_mul_f32 v214, s51, v134\n"
    "v_mul_f32 v215, s51, v135\n"
    "v_exp_f32 v212, v212\n"
    "ds_read_b128 a[176:179], v234 offset:2048\n"
    "ds_read_b128 a[180:183], v235 offset:2048\n"
    "s_waitcnt vmcnt(27) lgkmcnt(10)\n"
    "ds_read_b64_tr_b16 v[192:193], v229 offset:24576\n"
    "ds_read_b64_tr_b16 v[194:195], v229 offset:25600\n"
    "v_mfma_f32_32x32x16_bf16 v[32:47], v[176:179], a[112:115], v[32:47]\n"
    "buffer_load_dwordx4 v225, s[28:31], s49 offen offset:1024 lds\n"
    "v_exp_f32 v213, v213\n"
    "v_exp_f32 v214, v214\n"
    "ds_read_b128 a[184:187], v234 offset:3072\n"
    "ds_read_b128 a[188:191], v235 offset:3072\n"
    "v_mfma_f32_32x32x16_bf16 v[48:63], v[176:179], a[116:119], v[48:63]\n"
    "v_exp_f32 v215, v215\n"
    "v_add_f32 v212, 1.0, v212\n"
    "v_add_f32 v213, 1.0, v213\n"
    "v_add_f32 v214, 1.0, v214\n"
    "v_add_f32 v215, 1.0, v215\n"
    "ds_read_b128 a[192:195], v234 offset:4096\n"
    "ds_read_b128 a[196:199], v235 offset:4096\n"
    "ds_read_b64_tr_b16 v[196:197], v246 offset:24576\n"
    "ds_read_b64_tr_b16 v[198:199], v246 offset:25600\n"
    "v_mfma_f32_32x32x16_bf16 v[64:79], v[180:183], a[112:115], v[64:79]\n"
    "s_add_u32 m0, s48, 0x22000\n"
    "s_add_u32 s49, s63, 0x43800\n"
    "buffer_load_dwordx4 v224, s[28:31], s49 offen lds\n"
    "v_rcp_f32 v212, v212\n"
    "v_rcp_f32 v213, v213\n"
    "ds_read_b128 a[200:203], v234 offset:5120\n"
    "ds_read_b128 a[204:207], v235 offset:5120\n"
    "v_mfma_f32_32x32x16_bf16 v[80:95], v[180:183], a[116:119], v[80:95]\n"
    "v_rcp_f32 v214, v214\n"
    "v_rcp_f32 v215, v215\n"
    "v_mul_f32 v212, v132, v212\n"
    "v_mul_f32 v213, v133, v213\n"
    "ds_read_b128 a[208:211], v234 offset:6144\n"
    "ds_read_b128 a[212:215], v235 offset:6144\n"
    "s_waitcnt vmcnt(27) lgkmcnt(13)\n"
    "ds_read_b64_tr_b16 v[200:201], v228 offset:8192\n"
    "ds_read_b64_tr_b16 v[202:203], v228 offset:9216\n"
    "v_mfma_f32_32x32x16_bf16 v[32:47], v[184:187], a[120:123], v[32:47]\n"
    "buffer_load_dwordx4 v225, s[28:31], s49 offen offset:1024 lds\n"
    "v_mul_f32 v214, v134, v214\n"
    "v_mul_f32 v215, v135, v215\n"
    "v_cvt_pk_bf16_f32 v130, v212, v213\n"
    "v_cvt_pk_bf16_f32 v131, v214, v215\n"
    "ds_read_b128 a[216:219], v234 offset:7168\n"
    "ds_read_b128 a[220:223], v235 offset:7168\n"
    "v_mfma_f32_32x32x16_bf16 v[48:63], v[184:187], a[124:127], v[48:63]\n"
    "v_mul_f32 v208, s51, v136\n"
    "v_mul_f32 v209, s51, v137\n"
    "v_mul_f32 v210, s51, v138\n"
    "v_mul_f32 v211, s51, v139\n"
    "v_exp_f32 v208, v208\n"
    "ds_read_b64_tr_b16 v[204:205], v232 offset:8192\n"
    "ds_read_b64_tr_b16 v[206:207], v232 offset:9216\n"
    "v_mfma_f32_32x32x16_bf16 v[64:79], v[188:191], a[120:123], v[64:79]\n"
    "s_add_u32 m0, s48, 0\n"
    "s_add_u32 s49, s63, 0x63000\n"
    "buffer_load_dwordx4 v224, s[28:31], s49 offen lds\n"
    "v_exp_f32 v209, v209\n"
    "v_exp_f32 v210, v210\n"
    "v_mfma_f32_32x32x16_bf16 v[80:95], v[188:191], a[124:127], v[80:95]\n"
    "v_exp_f32 v211, v211\n"
    "v_add_f32 v208, 1.0, v208\n"
    "v_add_f32 v209, 1.0, v209\n"
    "v_add_f32 v210, 1.0, v210\n"
    "v_add_f32 v211, 1.0, v211\n"
    "s_waitcnt vmcnt(27) lgkmcnt(10)\n"
    "ds_read_b64_tr_b16 v[176:177], v228 offset:40960\n"
    "ds_read_b64_tr_b16 v[178:179], v228 offset:41984\n"
    "v_mfma_f32_32x32x16_bf16 v[32:47], v[192:195], a[128:131], v[32:47]\n"
    "buffer_load_dwordx4 v225, s[28:31], s49 offen offset:1024 lds\n"
    "v_rcp_f32 v208, v208\n"
    "v_rcp_f32 v209, v209\n"
    "v_mfma_f32_32x32x16_bf16 v[48:63], v[192:195], a[132:135], v[48:63]\n"
    "v_rcp_f32 v210, v210\n"
    "v_rcp_f32 v211, v211\n"
    "v_mul_f32 v208, v136, v208\n"
    "v_mul_f32 v209, v137, v209\n"
    "ds_read_b64_tr_b16 v[180:181], v232 offset:40960\n"
    "ds_read_b64_tr_b16 v[182:183], v232 offset:41984\n"
    "v_mfma_f32_32x32x16_bf16 v[64:79], v[196:199], a[128:131], v[64:79]\n"
    "s_add_u32 m0, s48, 0x10000\n"
    "s_add_u32 s49, s63, 0x67800\n"
    "buffer_load_dwordx4 v224, s[28:31], s49 offen lds\n"
    "v_mul_f32 v210, v138, v210\n"
    "v_mul_f32 v211, v139, v211\n"
    "v_cvt_pk_bf16_f32 v132, v208, v209\n"
    "v_cvt_pk_bf16_f32 v133, v210, v211\n"
    "s_waitcnt lgkmcnt(6)\n"
    "s_barrier\n"
    "v_mfma_f32_32x32x16_bf16 v[80:95], v[196:199], a[132:135], v[80:95]\n"
    "v_mul_f32 v212, s51, v140\n"
    "v_mul_f32 v213, s51, v141\n"
    "v_mul_f32 v214, s51, v142\n"
    "v_mul_f32 v215, s51, v143\n"
    "v_exp_f32 v212, v212\n"
    "s_waitcnt vmcnt(27) lgkmcnt(4)\n"
    "ds_read_b64_tr_b16 v[184:185], v228 offset:49152\n"
    "ds_read_b64_tr_b16 v[186:187], v228 offset:50176\n"
    "v_mfma_f32_32x32x16_bf16 v[32:47], v[200:203], a[136:139], v[32:47]\n"
    "buffer_load_dwordx4 v225, s[28:31], s49 offen offset:1024 lds\n"
    "v_exp_f32 v213, v213\n"
    "v_exp_f32 v214, v214\n"
    "v_mfma_f32_32x32x16_bf16 v[48:63], v[200:203], a[140:143], v[48:63]\n"
    "v_exp_f32 v215, v215\n"
    "v_add_f32 v212, 1.0, v212\n"
    "v_add_f32 v213, 1.0, v213\n"
    "v_add_f32 v214, 1.0, v214\n"
    "v_add_f32 v215, 1.0, v215\n"
    "ds_read_b64_tr_b16 v[188:189], v232 offset:49152\n"
    "ds_read_b64_tr_b16 v[190:191], v232 offset:50176\n"
    "v_mfma_f32_32x32x16_bf16 v[64:79], v[204:207], a[136:139], v[64:79]\n"
    "s_add_u32 m0, s48, 0x16000\n"
    "s_add_u32 s49, s63, 0x87000\n"
    "buffer_load_dwordx4 v224, s[28:31], s49 offen lds\n"
    "v_rcp_f32 v212, v212\n"
    "v_rcp_f32 v213, v213\n"
    "v_mfma_f32_32x32x16_bf16 v[80:95], v[204:207], a[140:143], v[80:95]\n"
    "v_rcp_f32 v214, v214\n"
    "v_rcp_f32 v215, v215\n"
    "v_mul_f32 v212, v140, v212\n"
    "v_mul_f32 v213, v141, v213\n"
    "s_waitcnt vmcnt(27) lgkmcnt(4)\n"
    "ds_read_b64_tr_b16 v[192:193], v229 offset:8192\n"
    "ds_read_b64_tr_b16 v[194:195], v229 offset:9216\n"
    "v_mfma_f32_32x32x16_bf16 v[32:47], v[176:179], a[144:147], v[32:47]\n"
    "buffer_load_dwordx4 v225, s[28:31], s49 offen offset:1024 lds\n"
    "v_mul_f32 v214, v142, v214\n"
    "v_mul_f32 v215, v143, v215\n"
    "v_cvt_pk_bf16_f32 v134, v212, v213\n"
    "v_cvt_pk_bf16_f32 v135, v214, v215\n"
    "ds_write_b128 v236, v[128:131] offset:0\n"
    "ds_write_b128 v236, v[132:135] offset:1024\n"
    "v_mfma_f32_32x32x16_bf16 v[48:63], v[176:179], a[148:151], v[48:63]\n"
    "v_mul_f32 v208, s51, v144\n"
    "v_mul_f32 v209, s51, v145\n"
    "v_mul_f32 v210, s51, v146\n"
    "v_mul_f32 v211, s51, v147\n"
    "v_exp_f32 v208, v208\n"
    "ds_read_b64_tr_b16 v[196:197], v246 offset:8192\n"
    "ds_read_b64_tr_b16 v[198:199], v246 offset:9216\n"
    "v_mfma_f32_32x32x16_bf16 v[64:79], v[180:183], a[144:147], v[64:79]\n"
    "s_add_u32 m0, s48, 0x2000\n"
    "s_add_u32 s49, s63, 0x8b800\n"
    "buffer_load_dwordx4 v224, s[28:31], s49 offen lds\n"
    "v_exp_f32 v209, v209\n"
    "v_exp_f32 v210, v210\n"
    "v_mfma_f32_32x32x16_bf16 v[80:95], v[180:183], a[148:151], v[80:95]\n"
    "v_exp_f32 v211, v211\n"
    "v_add_f32 v208, 1.0, v208\n"
    "v_add_f32 v209, 1.0, v209\n"
    "v_add_f32 v210, 1.0, v210\n"
    "v_add_f32 v211, 1.0, v211\n"
    "s_waitcnt vmcnt(27) lgkmcnt(6)\n"
    "ds_read_b64_tr_b16 v[200:201], v229 offset:57344\n"
    "ds_read_b64_tr_b16 v[202:203], v229 offset:58368\n"
    "v_mfma_f32_32x32x16_bf16 v[32:47], v[184:187], a[152:155], v[32:47]\n"
    "buffer_load_dwordx4 v225, s[28:31], s49 offen offset:1024 lds\n"
    "v_rcp_f32 v208, v208\n"
    "v_rcp_f32 v209, v209\n"
    "v_mfma_f32_32x32x16_bf16 v[48:63], v[184:187], a[156:159], v[48:63]\n"
    "v_rcp_f32 v210, v210\n"
    "v_rcp_f32 v211, v211\n"
    "v_mul_f32 v208, v144, v208\n"
    "v_mul_f32 v209, v145, v209\n"
    "ds_read_b64_tr_b16 v[204:205], v246 offset:57344\n"
    "ds_read_b64_tr_b16 v[206:207], v246 offset:58368\n"
    "v_mfma_f32_32x32x16_bf16 v[64:79], v[188:191], a[152:155], v[64:79]\n"
    "s_add_u32 m0, s48, 0xa000\n"
    "s_add_u32 s49, s46, s44\n"
    "s_add_u32 s49, s49, 4\n"
    "s_and_b32 s49, s49, 7\n"
    "s_lshl_b32 s49, s49, 7\n"
    "v_add_u32 v224, s49, v241\n"
    "v_add_u32 v225, 0x2000, v224\n"
    "s_add_u32 s49, s63, 0\n"
    "buffer_load_dwordx4 v224, s[28:31], s49 offen lds\n"
    "v_mul_f32 v210, v146, v210\n"
    "v_mul_f32 v211, v147, v211\n"
    "v_cvt_pk_bf16_f32 v144, v208, v209\n"
    "v_cvt_pk_bf16_f32 v145, v210, v211\n"
    "v_mfma_f32_32x32x16_bf16 v[80:95], v[188:191], a[156:159], v[80:95]\n"
    "v_mul_f32 v212, s51, v148\n"
    "v_mul_f32 v213, s51, v149\n"
    "v_mul_f32 v214, s51, v150\n"
    "v_mul_f32 v215, s51, v151\n"
    "v_exp_f32 v212, v212\n"
    "s_waitcnt vmcnt(27) lgkmcnt(4)\n"
    "ds_read_b64_tr_b16 v[176:177], v228 offset:16384\n"
    "ds_read_b64_tr_b16 v[178:179], v228 offset:17408\n"
    "v_mfma_f32_32x32x16_bf16 v[32:47], v[192:195], a[160:163], v[32:47]\n"
    "buffer_load_dwordx4 v225, s[28:31], s49 offen offset:1024 lds\n"
    "v_exp_f32 v213, v213\n"
    "v_exp_f32 v214, v214\n"
    "v_mfma_f32_32x32x16_bf16 v[48:63], v[192:195], a[164:167], v[48:63]\n"
    "v_exp_f32 v215, v215\n"
    "v_add_f32 v212, 1.0, v212\n"
    "v_add_f32 v213, 1.0, v213\n"
    "v_add_f32 v214, 1.0, v214\n"
    "v_add_f32 v215, 1.0, v215\n"
    "ds_read_b64_tr_b16 v[180:181], v232 offset:16384\n"
    "ds_read_b64_tr_b16 v[182:183], v232 offset:17408\n"
    "v_mfma_f32_32x32x16_bf16 v[64:79], v[196:199], a[160:163], v[64:79]\n"
    "s_add_u32 m0, s48, 0xc000\n"
    "s_add_u32 s49, s63, 0x4800\n"
    "buffer_load_dwordx4 v224, s[28:31], s49 offen lds\n"
    "v_rcp_f32 v212, v212\n"
    "v_rcp_f32 v213, v213\n"
    "v_mfma_f32_32x32x16_bf16 v[80:95], v[196:199], a[164:167], v[80:95]\n"
    "v_rcp_f32 v214, v214\n"
    "v_rcp_f32 v215, v215\n"
    "v_mul_f32 v212, v148, v212\n"
    "v_mul_f32 v213, v149, v213\n"
    "s_waitcnt vmcnt(27) lgkmcnt(4)\n"
    "ds_read_b64_tr_b16 v[184:185], v228 offset:24576\n"
    "ds_read_b64_tr_b16 v[186:187], v228 offset:25600\n"
    "v_mfma_f32_32x32x16_bf16 v[32:47], v[200:203], a[168:171], v[32:47]\n"
    "buffer_load_dwordx4 v225, s[28:31], s49 offen offset:1024 lds\n"
    "v_mul_f32 v214, v150, v214\n"
    "v_mul_f32 v215, v151, v215\n"
    "v_cvt_pk_bf16_f32 v146, v212, v213\n"
    "v_cvt_pk_bf16_f32 v147, v214, v215\n"
    "v_mfma_f32_32x32x16_bf16 v[48:63], v[200:203], a[172:175], v[48:63]\n"
    "v_mul_f32 v208, s51, v152\n"
    "v_mul_f32 v209, s51, v153\n"
    "v_mul_f32 v210, s51, v154\n"
    "v_mul_f32 v211, s51, v155\n"
    "v_exp_f32 v208, v208\n"
    "ds_read_b64_tr_b16 v[188:189], v232 offset:24576\n"
    "ds_read_b64_tr_b16 v[190:191], v232 offset:25600\n"
    "v_mfma_f32_32x32x16_bf16 v[64:79], v[204:207], a[168:171], v[64:79]\n"
    "s_add_u32 m0, s48, 0x12000\n"
    "s_add_u32 s49, s63, 0x24000\n"
    "buffer_load_dwordx4 v224, s[28:31], s49 offen lds\n"
    "v_exp_f32 v209, v209\n"
    "v_exp_f32 v210, v210\n"
    "v_mfma_f32_32x32x16_bf16 v[80:95], v[204:207], a[172:175], v[80:95]\n"
    "v_exp_f32 v211, v211\n"
    "v_add_f32 v208, 1.0, v208\n"
    "v_add_f32 v209, 1.0, v209\n"
    "v_add_f32 v210, 1.0, v210\n"
    "v_add_f32 v211, 1.0, v211\n"
    "s_waitcnt vmcnt(27) lgkmcnt(4)\n"
    "ds_read_b64_tr_b16 v[192:193], v229 offset:32768\n"
    "ds_read_b64_tr_b16 v[194:195], v229 offset:33792\n"
    "v_mfma_f32_32x32x16_bf16 v[32:47], v[176:179], a[176:179], v[32:47]\n"
    "buffer_load_dwordx4 v225, s[28:31], s49 offen offset:1024 lds\n"
    "v_rcp_f32 v208, v208\n"
    "v_rcp_f32 v209, v209\n"
    "v_mfma_f32_32x32x16_bf16 v[48:63], v[176:179], a[180:183], v[48:63]\n"
    "v_rcp_f32 v210, v210\n"
    "v_rcp_f32 v211, v211\n"
    "v_mul_f32 v208, v152, v208\n"
    "v_mul_f32 v209, v153, v209\n"
    "ds_read_b64_tr_b16 v[196:197], v246 offset:32768\n"
    "ds_read_b64_tr_b16 v[198:199], v246 offset:33792\n"
    "v_mfma_f32_32x32x16_bf16 v[64:79], v[180:183], a[176:179], v[64:79]\n"
    "s_add_u32 m0, s48, 0x1e000\n"
    "s_add_u32 s49, s63, 0x28800\n"
    "buffer_load_dwordx4 v224, s[28:31], s49 offen lds\n"
    "v_mul_f32 v210, v154, v210\n"
    "v_mul_f32 v211, v155, v211\n"
    "v_cvt_pk_bf16_f32 v148, v208, v209\n"
    "v_cvt_pk_bf16_f32 v149, v210, v211\n"
    "v_mfma_f32_32x32x16_bf16 v[80:95], v[180:183], a[180:183], v[80:95]\n"
    "v_mul_f32 v212, s51, v156\n"
    "v_mul_f32 v213, s51, v157\n"
    "v_mul_f32 v214, s51, v158\n"
    "v_mul_f32 v215, s51, v159\n"
    "v_exp_f32 v212, v212\n"
    "s_waitcnt vmcnt(27) lgkmcnt(4)\n"
    "ds_read_b64_tr_b16 v[200:201], v229 offset:40960\n"
    "ds_read_b64_tr_b16 v[202:203], v229 offset:41984\n"
    "v_mfma_f32_32x32x16_bf16 v[32:47], v[184:187], a[184:187], v[32:47]\n"
    "buffer_load_dwordx4 v225, s[28:31], s49 offen offset:1024 lds\n"
    "v_exp_f32 v213, v213\n"
    "v_exp_f32 v214, v214\n"
    "v_mfma_f32_32x32x16_bf16 v[48:63], v[184:187], a[188:191], v[48:63]\n"
    "v_exp_f32 v215, v215\n"
    "v_add_f32 v212, 1.0, v212\n"
    "v_add_f32 v213, 1.0, v213\n"
    "v_add_f32 v214, 1.0, v214\n"
    "v_add_f32 v215, 1.0, v215\n"
    "ds_read_b64_tr_b16 v[204:205], v246 offset:40960\n"
    "ds_read_b64_tr_b16 v[206:207], v246 offset:41984\n"
    "v_mfma_f32_32x32x16_bf16 v[64:79], v[188:191], a[184:187], v[64:79]\n"
    "s_add_u32 m0, s48, 0x4000\n"
    "s_add_u32 s49, s63, 0x48000\n"
    "buffer_load_dwordx4 v224, s[28:31], s49 offen lds\n"
    "v_rcp_f32 v212, v212\n"
    "v_rcp_f32 v213, v213\n"
    "v_mfma_f32_32x32x16_bf16 v[80:95], v[188:191], a[188:191], v[80:95]\n"
    "v_rcp_f32 v214, v214\n"
    "v_rcp_f32 v215, v215\n"
    "v_mul_f32 v212, v156, v212\n"
    "v_mul_f32 v213, v157, v213\n"
    "s_waitcnt vmcnt(27) lgkmcnt(4)\n"
    "ds_read_b64_tr_b16 v[176:177], v231 offset:0\n"
    "ds_read_b64_tr_b16 v[178:179], v231 offset:1024\n"
    "v_mfma_f32_32x32x16_bf16 v[32:47], v[192:195], a[192:195], v[32:47]\n"
    "buffer_load_dwordx4 v225, s[28:31], s49 offen offset:1024 lds\n"
    "v_mul_f32 v214, v158, v214\n"
    "v_mul_f32 v215, v159, v215\n"
    "v_cvt_pk_bf16_f32 v150, v212, v213\n"
    "v_cvt_pk_bf16_f32 v151, v214, v215\n"
    "ds_write_b128 v237, v[144:147] offset:0\n"
    "ds_write_b128 v237, v[148:151] offset:1024\n"
    "v_mfma_f32_32x32x16_bf16 v[48:63], v[192:195], a[196:199], v[48:63]\n"
    "ds_read_b64_tr_b16 v[180:181], v247 offset:0\n"
    "ds_read_b64_tr_b16 v[182:183], v247 offset:1024\n"
    "v_mfma_f32_32x32x16_bf16 v[64:79], v[196:199], a[192:195], v[64:79]\n"
    "s_add_u32 m0, s48, 0x6000\n"
    "s_add_u32 s49, s63, 0x4c800\n"
    "buffer_load_dwordx4 v224, s[28:31], s49 offen lds\n"
    "v_mfma_f32_32x32x16_bf16 v[80:95], v[196:199], a[196:199], v[80:95]\n"
    "s_waitcnt vmcnt(27) lgkmcnt(6)\n"
    "ds_read_b64_tr_b16 v[184:185], v228 offset:32768\n"
    "ds_read_b64_tr_b16 v[186:187], v228 offset:33792\n"
    "v_mfma_f32_32x32x16_bf16 v[32:47], v[200:203], a[200:203], v[32:47]\n"
    "buffer_load_dwordx4 v225, s[28:31], s49 offen offset:1024 lds\n"
    "s_waitcnt lgkmcnt(4)\n"
    "s_barrier\n"
    "v_mfma_f32_32x32x16_bf16 v[48:63], v[200:203], a[204:207], v[48:63]\n"
    "ds_read_b128 a[224:227], v234 offset:0\n"
    "ds_read_b128 a[228:231], v235 offset:0\n"
    "ds_read_b64_tr_b16 v[188:189], v232 offset:32768\n"
    "ds_read_b64_tr_b16 v[190:191], v232 offset:33792\n"
    "v_mfma_f32_32x32x16_bf16 v[64:79], v[204:207], a[200:203], v[64:79]\n"
    "s_add_u32 m0, s48, 0x18000\n"
    "s_add_u32 s49, s63, 0x6c000\n"
    "buffer_load_dwordx4 v224, s[28:31], s49 offen lds\n"
    "ds_read_b128 a[232:235], v234 offset:1024\n"
    "ds_read_b128 a[236:239], v235 offset:1024\n"
    "v_mfma_f32_32x32x16_bf16 v[80:95], v[204:207], a[204:207], v[80:95]\n"
    "ds_read_b128 a[240:243], v234 offset:2048\n"
    "ds_read_b128 a[244:247], v235 offset:2048\n"
    "s_waitcnt vmcnt(27) lgkmcnt(10)\n"
    "ds_read_b64_tr_b16 v[192:193], v228 offset:57344\n"
    "ds_read_b64_tr_b16 v[194:195], v228 offset:58368\n"
    "v_mfma_f32_32x32x16_bf16 v[32:47], v[176:179], a[208:211], v[32:47]\n"
    "buffer_load_dwordx4 v225, s[28:31], s49 offen offset:1024 lds\n"
    "ds_read_b128 a[248:251], v234 offset:3072\n"
    "ds_read_b128 a[252:255], v235 offset:3072\n"
    "v_mfma_f32_32x32x16_bf16 v[48:63], v[176:179], a[212:215], v[48:63]\n"
    "ds_read_b128 v[0:3], v234 offset:4096\n"
    "ds_read_b128 v[4:7], v235 offset:4096\n"
    "ds_read_b64_tr_b16 v[196:197], v232 offset:57344\n"
    "ds_read_b64_tr_b16 v[198:199], v232 offset:58368\n"
    "v_mfma_f32_32x32x16_bf16 v[64:79], v[180:183], a[208:211], v[64:79]\n"
    "s_add_u32 m0, s48, 0x1a000\n"
    "s_add_u32 s49, s63, 0x70800\n"
    "buffer_load_dwordx4 v224, s[28:31], s49 offen lds\n"
    "ds_read_b128 v[8:11], v234 offset:5120\n"
    "ds_read_b128 v[12:15], v235 offset:5120\n"
    "v_mfma_f32_32x32x16_bf16 v[80:95], v[180:183], a[212:215], v[80:95]\n"
    "ds_read_b128 v[16:19], v234 offset:6144\n"
    "ds_read_b128 v[20:23], v235 offset:6144\n"
    "s_waitcnt vmcnt(27) lgkmcnt(13)\n"
    "ds_read_b64_tr_b16 v[200:201], v229 offset:16384\n"
    "ds_read_b64_tr_b16 v[202:203], v229 offset:17408\n"
    "v_mfma_f32_32x32x16_bf16 v[32:47], v[184:187], a[216:219], v[32:47]\n"
    "buffer_load_dwordx4 v225, s[28:31], s49 offen offset:1024 lds\n"
    "ds_read_b128 v[24:27], v234 offset:7168\n"
    "ds_read_b128 v[28:31], v235 offset:7168\n"
    "v_mfma_f32_32x32x16_bf16 v[48:63], v[184:187], a[220:223], v[48:63]\n"
    "ds_read_b64_tr_b16 v[204:205], v246 offset:16384\n"
    "ds_read_b64_tr_b16 v[206:207], v246 offset:17408\n"
    "v_mfma_f32_32x32x16_bf16 v[64:79], v[188:191], a[216:219], v[64:79]\n"
    "s_add_u32 m0, s48, 0x20000\n"
    "s_add_u32 s49, s63, 0x9000\n"
    "buffer_load_dwordx4 v224, s[28:31], s49 offen lds\n"
    "v_mfma_f32_32x32x16_bf16 v[80:95], v[188:191], a[220:223], v[80:95]\n"
    "s_waitcnt vmcnt(27) lgkmcnt(10)\n"
    "ds_read_b64_tr_b16 v[176:177], v229 offset:49152\n"
    "ds_read_b64_tr_b16 v[178:179], v229 offset:50176\n"
    "v_mfma_f32_32x32x16_bf16 v[32:47], v[192:195], a[224:227], v[32:47]\n"
    "buffer_load_dwordx4 v225, s[28:31], s49 offen offset:1024 lds\n"
    "v_mfma_f32_32x32x16_bf16 v[48:63], v[192:195], a[228:231], v[48:63]\n"
    "ds_read_b64_tr_b16 v[180:181], v246 offset:49152\n"
    "ds_read_b64_tr_b16 v[182:183], v246 offset:50176\n"
    "v_mfma_f32_32x32x16_bf16 v[64:79], v[196:199], a[224:227], v[64:79]\n"
    "s_add_u32 m0, s48, 0x8000\n"
    "s_add_u32 s49, s63, 0xd800\n"
    "buffer_load_dwordx4 v224, s[28:31], s49 offen lds\n"
    "v_mfma_f32_32x32x16_bf16 v[80:95], v[196:199], a[228:231], v[80:95]\n"
    "s_waitcnt vmcnt(27) lgkmcnt(4)\n"
    "ds_read_b64_tr_b16 v[184:185], v231 offset:8192\n"
    "ds_read_b64_tr_b16 v[186:187], v231 offset:9216\n"
    "v_mfma_f32_32x32x16_bf16 v[32:47], v[200:203], a[232:235], v[32:47]\n"
    "buffer_load_dwordx4 v225, s[28:31], s49 offen offset:1024 lds\n"
    "v_mfma_f32_32x32x16_bf16 v[48:63], v[200:203], a[236:239], v[48:63]\n"
    "ds_read_b64_tr_b16 v[188:189], v247 offset:8192\n"
    "ds_read_b64_tr_b16 v[190:191], v247 offset:9216\n"
    "v_mfma_f32_32x32x16_bf16 v[64:79], v[204:207], a[232:235], v[64:79]\n"
    "s_add_u32 m0, s48, 0xe000\n"
    "s_add_u32 s49, s63, 0x2d000\n"
    "buffer_load_dwordx4 v224, s[28:31], s49 offen lds\n"
    "v_mfma_f32_32x32x16_bf16 v[80:95], v[204:207], a[236:239], v[80:95]\n"
    "s_waitcnt vmcnt(27) lgkmcnt(4)\n"
    "ds_read_b64_tr_b16 v[192:193], v228 offset:0\n"
    "ds_read_b64_tr_b16 v[194:195], v228 offset:1024\n"
    "v_mfma_f32_32x32x16_bf16 v[32:47], v[176:179], a[240:243], v[32:47]\n"
    "buffer_load_dwordx4 v225, s[28:31], s49 offen offset:1024 lds\n"
    "v_mfma_f32_32x32x16_bf16 v[48:63], v[176:179], a[244:247], v[48:63]\n"
    "ds_read_b64_tr_b16 v[196:197], v232 offset:0\n"
    "ds_read_b64_tr_b16 v[198:199], v232 offset:1024\n"
    "v_mfma_f32_32x32x16_bf16 v[64:79], v[180:183], a[240:243], v[64:79]\n"
    "s_add_u32 m0, s48, 0x14000\n"
    "s_add_u32 s49, s63, 0x31800\n"
    "buffer_load_dwordx4 v224, s[28:31], s49 offen lds\n"
    "v_mfma_f32_32x32x16_bf16 v[80:95], v[180:183], a[244:247], v[80:95]\n"
    "s_waitcnt vmcnt(27) lgkmcnt(4)\n"
    "ds_read_b64_tr_b16 v[200:201], v229 offset:0\n"
    "ds_read_b64_tr_b16 v[202:203], v229 offset:1024\n"
    "v_mfma_f32_32x32x16_bf16 v[32:47], v[184:187], a[248:251], v[32:47]\n"
    "buffer_load_dwordx4 v225, s[28:31], s49 offen offset:1024 lds\n"
    "v_mfma_f32_32x32x16_bf16 v[48:63], v[184:187], a[252:255], v[48:63]\n"
    "ds_read_b64_tr_b16 v[204:205], v246 offset:0\n"
    "ds_read_b64_tr_b16 v[206:207], v246 offset:1024\n"
    "v_mfma_f32_32x32x16_bf16 v[64:79], v[188:191], a[248:251], v[64:79]\n"
    "s_add_u32 m0, s48, 0x1c000\n"
    "s_add_u32 s49, s63, 0x51000\n"
    "buffer_load_dwordx4 v224, s[28:31], s49 offen lds\n"
    "v_mfma_f32_32x32x16_bf16 v[80:95], v[188:191], a[252:255], v[80:95]\n"
    "s_waitcnt vmcnt(27) lgkmcnt(4)\n"
    "ds_read_b64_tr_b16 v[176:177], v229 offset:24576\n"
    "ds_read_b64_tr_b16 v[178:179], v229 offset:25600\n"
    "v_mfma_f32_32x32x16_bf16 v[32:47], v[192:195], v[0:3], v[32:47]\n"
    "buffer_load_dwordx4 v225, s[28:31], s49 offen offset:1024 lds\n"
    "v_mfma_f32_32x32x16_bf16 v[48:63], v[192:195], v[4:7], v[48:63]\n"
    "ds_read_b64_tr_b16 v[180:181], v246 offset:24576\n"
    "ds_read_b64_tr_b16 v[182:183], v246 offset:25600\n"
    "v_mfma_f32_32x32x16_bf16 v[64:79], v[196:199], v[0:3], v[64:79]\n"
    "s_add_u32 m0, s48, 0x22000\n"
    "s_add_u32 s49, s63, 0x55800\n"
    "buffer_load_dwordx4 v224, s[28:31], s49 offen lds\n"
    "v_mfma_f32_32x32x16_bf16 v[80:95], v[196:199], v[4:7], v[80:95]\n"
    "s_waitcnt vmcnt(27) lgkmcnt(4)\n"
    "ds_read_b64_tr_b16 v[184:185], v228 offset:8192\n"
    "ds_read_b64_tr_b16 v[186:187], v228 offset:9216\n"
    "v_mfma_f32_32x32x16_bf16 v[32:47], v[200:203], v[8:11], v[32:47]\n"
    "buffer_load_dwordx4 v225, s[28:31], s49 offen offset:1024 lds\n"
    "v_mfma_f32_32x32x16_bf16 v[48:63], v[200:203], v[12:15], v[48:63]\n"
    "ds_read_b64_tr_b16 v[188:189], v232 offset:8192\n"
    "ds_read_b64_tr_b16 v[190:191], v232 offset:9216\n"
    "v_mfma_f32_32x32x16_bf16 v[64:79], v[204:207], v[8:11], v[64:79]\n"
    "s_add_u32 m0, s48, 0\n"
    "s_add_u32 s49, s63, 0x75000\n"
    "buffer_load_dwordx4 v224, s[28:31], s49 offen lds\n"
    "v_mfma_f32_32x32x16_bf16 v[80:95], v[204:207], v[12:15], v[80:95]\n"
    "s_waitcnt vmcnt(27) lgkmcnt(4)\n"
    "ds_read_b64_tr_b16 v[192:193], v228 offset:40960\n"
    "ds_read_b64_tr_b16 v[194:195], v228 offset:41984\n"
    "v_mfma_f32_32x32x16_bf16 v[32:47], v[176:179], v[16:19], v[32:47]\n"
    "buffer_load_dwordx4 v225, s[28:31], s49 offen offset:1024 lds\n"
    "v_mfma_f32_32x32x16_bf16 v[48:63], v[176:179], v[20:23], v[48:63]\n"
    "ds_read_b64_tr_b16 v[196:197], v232 offset:40960\n"
    "ds_read_b64_tr_b16 v[198:199], v232 offset:41984\n"
    "v_mfma_f32_32x32x16_bf16 v[64:79], v[180:183], v[16:19], v[64:79]\n"
    "s_add_u32 m0, s48, 0x10000\n"
    "s_add_u32 s49, s63, 0x79800\n"
    "buffer_load_dwordx4 v224, s[28:31], s49 offen lds\n"
    "v_mfma_f32_32x32x16_bf16 v[80:95], v[180:183], v[20:23], v[80:95]\n"
    "s_waitcnt vmcnt(27) lgkmcnt(4)\n"
    "ds_read_b64_tr_b16 v[200:201], v228 offset:49152\n"
    "ds_read_b64_tr_b16 v[202:203], v228 offset:50176\n"
    "v_mfma_f32_32x32x16_bf16 v[32:47], v[184:187], v[24:27], v[32:47]\n"
    "buffer_load_dwordx4 v225, s[28:31], s49 offen offset:1024 lds\n"
    "v_mfma_f32_32x32x16_bf16 v[48:63], v[184:187], v[28:31], v[48:63]\n"
    "ds_read_b64_tr_b16 v[204:205], v232 offset:49152\n"
    "ds_read_b64_tr_b16 v[206:207], v232 offset:50176\n"
    "v_mfma_f32_32x32x16_bf16 v[64:79], v[188:191], v[24:27], v[64:79]\n"
    "s_add_u32 m0, s48, 0x16000\n"
    "s_add_u32 s49, s63, 0x90000\n"
    "buffer_load_dwordx4 v224, s[28:31], s49 offen lds\n"
    "v_mfma_f32_32x32x16_bf16 v[80:95], v[188:191], v[28:31], v[80:95]\n"
    "s_waitcnt vmcnt(27) lgkmcnt(4)\n"
    "ds_read_b64_tr_b16 v[176:177], v229 offset:8192\n"
    "ds_read_b64_tr_b16 v[178:179], v229 offset:9216\n"
    "v_mfma_f32_32x32x16_bf16 v[96:111], v[192:195], a[0:3], 0\n"
    "buffer_load_dwordx4 v225, s[28:31], s49 offen offset:1024 lds\n"
    "v_mfma_f32_32x32x16_bf16 v[112:127], v[192:195], a[4:7], 0\n"
    "s_add_u32 s50, s46, s44\n"
    "s_and_b32 s50, s50, 7\n"
    "s_lshl_b32 s8, s45, 3\n"
    "s_add_u32 s50, s50, s8\n"
    "s_mul_i32 s50, s50, 7\n"
    "s_add_u32 s50, s50, s44\n"
    "s_add_u32 s50, s50, -1\n"
    "s_lshl_b32 s50, s50, 13\n"
    "s_movk_i32 s10, 0x400\n"
    "s_lshl_b32 s8, s45, 6\n"
    "s_add_u32 s8, s8, s46\n"
    "s_lshl_b32 s8, s8, 10\n"
    "s_add_u32 s8, s8, 0xe00000\n"
    "s_cmp_eq_u32 s44, 0\n"
    "s_cselect_b32 s50, s8, s50\n"
    "s_cselect_b32 s10, 0x2000, s10\n"
    "s_add_u32 s50, s50, s68\n"
    "s_lshl_b32 s9, s10, 1\n"
    "s_mul_i32 s8, s9, s47\n"
    "s_sub_u32 s9, s9, s8\n"
    "s_add_u32 s8, s8, s50\n"
    "s_add_u32 s9, s9, s50\n"
    "v_cvt_pk_bf16_f32 v32, v32, v33\n"
    "v_cvt_pk_bf16_f32 v33, v34, v35\n"
    "v_cvt_pk_bf16_f32 v34, v36, v37\n"
    "v_cvt_pk_bf16_f32 v35, v38, v39\n"
    "buffer_store_dwordx4 v[32:35], v239, s[32:35], s8 offen sc1\n"
    "ds_read_b64_tr_b16 v[180:181], v246 offset:8192\n"
    "ds_read_b64_tr_b16 v[182:183], v246 offset:9216\n"
    "v_mfma_f32_32x32x16_bf16 v[128:143], v[196:199], a[0:3], 0\n"
    "s_add_u32 m0, s48, 0x2000\n"
    "s_add_u32 s49, s63, 0x94800\n"
    "buffer_load_dwordx4 v224, s[28:31], s49 offen lds\n"
    "v_cvt_pk_bf16_f32 v36, v40, v41\n"
    "v_cvt_pk_bf16_f32 v37, v42, v43\n"
    "v_cvt_pk_bf16_f32 v38, v44, v45\n"
    "v_mfma_f32_32x32x16_bf16 v[144:159], v[196:199], a[4:7], 0\n"
    "v_cvt_pk_bf16_f32 v39, v46, v47\n"
    "s_mul_i32 s50, s10, 1\n"
    "s_add_u32 s50, s50, s8\n"
    "buffer_store_dwordx4 v[36:39], v239, s[32:35], s50 offen sc1\n"
    "v_cvt_pk_bf16_f32 v48, v48, v49\n"
    "v_cvt_pk_bf16_f32 v49, v50, v51\n"
    "v_cvt_pk_bf16_f32 v50, v52, v53\n"
    "s_waitcnt vmcnt(29) lgkmcnt(4)\n"
    "ds_read_b64_tr_b16 v[184:185], v229 offset:57344\n"
    "ds_read_b64_tr_b16 v[186:187], v229 offset:58368\n"
    "v_mfma_f32_32x32x16_bf16 v[96:111], v[200:203], a[8:11], v[96:111]\n"
    "buffer_load_dwordx4 v225, s[28:31], s49 offen offset:1024 lds\n"
    "v_cvt_pk_bf16_f32 v51, v54, v55\n"
    "buffer_store_dwordx4 v[48:51], v239, s[32:35], s9 offen sc1\n"
    "v_cvt_pk_bf16_f32 v52, v56, v57\n"
    "v_mfma_f32_32x32x16_bf16 v[112:127], v[200:203], a[12:15], v[112:127]\n"
    "v_cvt_pk_bf16_f32 v53, v58, v59\n"
    "v_cvt_pk_bf16_f32 v54, v60, v61\n"
    "v_cvt_pk_bf16_f32 v55, v62, v63\n"
    "s_mul_i32 s50, s10, 1\n"
    "s_add_u32 s50, s50, s9\n"
    "buffer_store_dwordx4 v[52:55], v239, s[32:35], s50 offen sc1\n"
    "v_cvt_pk_bf16_f32 v64, v64, v65\n"
    "ds_read_b64_tr_b16 v[188:189], v246 offset:57344\n"
    "ds_read_b64_tr_b16 v[190:191], v246 offset:58368\n"
    "v_mfma_f32_32x32x16_bf16 v[128:143], v[204:207], a[8:11], v[128:143]\n"
    "s_add_u32 m0, s48, 0xa000\n"
    "s_add_u32 s49, s63, 0x99000\n"
    "buffer_load_dwordx4 v224, s[28:31], s49 offen lds\n"
    "v_cvt_pk_bf16_f32 v65, v66, v67\n"
    "v_cvt_pk_bf16_f32 v66, v68, v69\n"
    "v_cvt_pk_bf16_f32 v67, v70, v71\n"
    "s_mul_i32 s50, s10, 4\n"
    "s_add_u32 s50, s50, s8\n"
    "buffer_store_dwordx4 v[64:67], v239, s[32:35], s50 offen sc1\n"
    "v_mfma_f32_32x32x16_bf16 v[144:159], v[204:207], a[12:15], v[144:159]\n"
    "v_cvt_pk_bf16_f32 v68, v72, v73\n"
    "v_cvt_pk_bf16_f32 v69, v74, v75\n"
    "v_cvt_pk_bf16_f32 v70, v76, v77\n"
    "v_cvt_pk_bf16_f32 v71, v78, v79\n"
    "s_mul_i32 s50, s10, 5\n"
    "s_add_u32 s50, s50, s8\n"
    "buffer_store_dwordx4 v[68:71], v239, s[32:35], s50 offen sc1\n"
    "s_waitcnt vmcnt(33) lgkmcnt(4)\n"
    "ds_read_b64_tr_b16 v[192:193], v228 offset:16384\n"
    "ds_read_b64_tr_b16 v[194:195], v228 offset:17408\n"
    "v_mfma_f32_32x32x16_bf16 v[96:111], v[176:179], a[16:19], v[96:111]\n"
    "buffer_load_dwordx4 v225, s[28:31], s49 offen offset:1024 lds\n"
    "v_cvt_pk_bf16_f32 v80, v80, v81\n"
    "v_cvt_pk_bf16_f32 v81, v82, v83\n"
    "v_cvt_pk_bf16_f32 v82, v84, v85\n"
    "v_mfma_f32_32x32x16_bf16 v[112:127], v[176:179], a[20:23], v[112:127]\n"
    "v_cvt_pk_bf16_f32 v83, v86, v87\n"
    "s_mul_i32 s50, s10, 4\n"
    "s_add_u32 s50, s50, s9\n"
    "buffer_store_dwordx4 v[80:83], v239, s[32:35], s50 offen sc1\n"
    "v_cvt_pk_bf16_f32 v84, v88, v89\n"
    "v_cvt_pk_bf16_f32 v85, v90, v91\n"
    "v_cvt_pk_bf16_f32 v86, v92, v93\n"
    "ds_read_b64_tr_b16 v[196:197], v232 offset:16384\n"
    "ds_read_b64_tr_b16 v[198:199], v232 offset:17408\n"
    "v_mfma_f32_32x32x16_bf16 v[128:143], v[180:183], a[16:19], v[128:143]\n"
    "s_add_u32 m0, s48, 0xc000\n"
    "s_add_u32 s49, s63, 0x9d800\n"
    "buffer_load_dwordx4 v224, s[28:31], s49 offen lds\n"
    "v_cvt_pk_bf16_f32 v87, v94, v95\n"
    "s_mul_i32 s50, s10, 5\n"
    "s_add_u32 s50, s50, s9\n"
    "buffer_store_dwordx4 v[84:87], v239, s[32:35], s50 offen sc1\n"
    "v_mfma_f32_32x32x16_bf16 v[144:159], v[180:183], a[20:23], v[144:159]\n"
    "s_waitcnt vmcnt(35) lgkmcnt(4)\n"
    "ds_read_b64_tr_b16 v[200:201], v228 offset:24576\n"
    "ds_read_b64_tr_b16 v[202:203], v228 offset:25600\n"
    "v_mfma_f32_32x32x16_bf16 v[96:111], v[184:187], a[24:27], v[96:111]\n"
    "buffer_load_dwordx4 v225, s[28:31], s49 offen offset:1024 lds\n"
    "v_mfma_f32_32x32x16_bf16 v[112:127], v[184:187], a[28:31], v[112:127]\n"
    "ds_read_b64_tr_b16 v[204:205], v232 offset:24576\n"
    "ds_read_b64_tr_b16 v[206:207], v232 offset:25600\n"
    "v_mfma_f32_32x32x16_bf16 v[128:143], v[188:191], a[24:27], v[128:143]\n"
    "s_add_u32 m0, s48, 0x12000\n"
    "s_add_u32 s49, s63, 0x12000\n"
    "buffer_load_dwordx4 v224, s[28:31], s49 offen lds\n"
    "v_mfma_f32_32x32x16_bf16 v[144:159], v[188:191], a[28:31], v[144:159]\n"
    "s_waitcnt vmcnt(35) lgkmcnt(4)\n"
    "ds_read_b64_tr_b16 v[176:177], v229 offset:32768\n"
    "ds_read_b64_tr_b16 v[178:179], v229 offset:33792\n"
    "v_mfma_f32_32x32x16_bf16 v[96:111], v[192:195], a[32:35], v[96:111]\n"
    "buffer_load_dwordx4 v225, s[28:31], s49 offen offset:1024 lds\n"
    "v_mfma_f32_32x32x16_bf16 v[112:127], v[192:195], a[36:39], v[112:127]\n"
    "ds_read_b64_tr_b16 v[180:181], v246 offset:32768\n"
    "ds_read_b64_tr_b16 v[182:183], v246 offset:33792\n"
    "v_mfma_f32_32x32x16_bf16 v[128:143], v[196:199], a[32:35], v[128:143]\n"
    "s_add_u32 m0, s48, 0x1e000\n"
    "s_add_u32 s49, s63, 0x16800\n"
    "buffer_load_dwordx4 v224, s[28:31], s49 offen lds\n"
    "v_mfma_f32_32x32x16_bf16 v[144:159], v[196:199], a[36:39], v[144:159]\n"
    "s_waitcnt vmcnt(35) lgkmcnt(4)\n"
    "ds_read_b64_tr_b16 v[184:185], v229 offset:40960\n"
    "ds_read_b64_tr_b16 v[186:187], v229 offset:41984\n"
    "v_mfma_f32_32x32x16_bf16 v[96:111], v[200:203], a[40:43], v[96:111]\n"
    "buffer_load_dwordx4 v225, s[28:31], s49 offen offset:1024 lds\n"
    "v_mfma_f32_32x32x16_bf16 v[112:127], v[200:203], a[44:47], v[112:127]\n"
    "ds_read_b64_tr_b16 v[188:189], v246 offset:40960\n"
    "ds_read_b64_tr_b16 v[190:191], v246 offset:41984\n"
    "v_mfma_f32_32x32x16_bf16 v[128:143], v[204:207], a[40:43], v[128:143]\n"
    "s_add_u32 m0, s48, 0x4000\n"
    "s_add_u32 s49, s63, 0x36000\n"
    "buffer_load_dwordx4 v224, s[28:31], s49 offen lds\n"
    "v_mfma_f32_32x32x16_bf16 v[144:159], v[204:207], a[44:47], v[144:159]\n"
    "s_waitcnt vmcnt(35) lgkmcnt(4)\n"
    "ds_read_b64_tr_b16 v[192:193], v231 offset:0\n"
    "ds_read_b64_tr_b16 v[194:195], v231 offset:1024\n"
    "v_mfma_f32_32x32x16_bf16 v[96:111], v[176:179], a[48:51], v[96:111]\n"
    "buffer_load_dwordx4 v225, s[28:31], s49 offen offset:1024 lds\n"
    "v_mfma_f32_32x32x16_bf16 v[112:127], v[176:179], a[52:55], v[112:127]\n"
    "ds_read_b64_tr_b16 v[196:197], v247 offset:0\n"
    "ds_read_b64_tr_b16 v[198:199], v247 offset:1024\n"
    "v_mfma_f32_32x32x16_bf16 v[128:143], v[180:183], a[48:51], v[128:143]\n"
    "s_add_u32 m0, s48, 0x6000\n"
    "s_add_u32 s49, s63, 0x3a800\n"
    "buffer_load_dwordx4 v224, s[28:31], s49 offen lds\n"
    "v_mfma_f32_32x32x16_bf16 v[144:159], v[180:183], a[52:55], v[144:159]\n"
    "s_waitcnt vmcnt(35) lgkmcnt(4)\n"
    "ds_read_b64_tr_b16 v[200:201], v228 offset:32768\n"
    "ds_read_b64_tr_b16 v[202:203], v228 offset:33792\n"
    "v_mfma_f32_32x32x16_bf16 v[96:111], v[184:187], a[56:59], v[96:111]\n"
    "buffer_load_dwordx4 v225, s[28:31], s49 offen offset:1024 lds\n"
    "v_mfma_f32_32x32x16_bf16 v[112:127], v[184:187], a[60:63], v[112:127]\n"
    "ds_read_b64_tr_b16 v[204:205], v232 offset:32768\n"
    "ds_read_b64_tr_b16 v[206:207], v232 offset:33792\n"
    "v_mfma_f32_32x32x16_bf16 v[128:143], v[188:191], a[56:59], v[128:143]\n"
    "s_add_u32 m0, s48, 0x18000\n"
    "s_add_u32 s49, s63, 0x5a000\n"
    "buffer_load_dwordx4 v224, s[28:31], s49 offen lds\n"
    "v_mfma_f32_32x32x16_bf16 v[144:159], v[188:191], a[60:63], v[144:159]\n"
    "s_waitcnt vmcnt(35) lgkmcnt(4)\n"
    "ds_read_b64_tr_b16 v[176:177], v228 offset:57344\n"
    "ds_read_b64_tr_b16 v[178:179], v228 offset:58368\n"
    "v_mfma_f32_32x32x16_bf16 v[96:111], v[192:195], a[64:67], v[96:111]\n"
    "buffer_load_dwordx4 v225, s[28:31], s49 offen offset:1024 lds\n"
    "v_mfma_f32_32x32x16_bf16 v[112:127], v[192:195], a[68:71], v[112:127]\n"
    "ds_read_b64_tr_b16 v[180:181], v232 offset:57344\n"
    "ds_read_b64_tr_b16 v[182:183], v232 offset:58368\n"
    "v_mfma_f32_32x32x16_bf16 v[128:143], v[196:199], a[64:67], v[128:143]\n"
    "s_add_u32 m0, s48, 0x1a000\n"
    "s_add_u32 s49, s63, 0x5e800\n"
    "buffer_load_dwordx4 v224, s[28:31], s49 offen lds\n"
    "v_mfma_f32_32x32x16_bf16 v[144:159], v[196:199], a[68:71], v[144:159]\n"
    "s_waitcnt vmcnt(35) lgkmcnt(4)\n"
    "ds_read_b64_tr_b16 v[184:185], v229 offset:16384\n"
    "ds_read_b64_tr_b16 v[186:187], v229 offset:17408\n"
    "v_mfma_f32_32x32x16_bf16 v[96:111], v[200:203], a[72:75], v[96:111]\n"
    "buffer_load_dwordx4 v225, s[28:31], s49 offen offset:1024 lds\n"
    "v_mfma_f32_32x32x16_bf16 v[112:127], v[200:203], a[76:79], v[112:127]\n"
    "ds_read_b64_tr_b16 v[188:189], v246 offset:16384\n"
    "ds_read_b64_tr_b16 v[190:191], v246 offset:17408\n"
    "v_mfma_f32_32x32x16_bf16 v[128:143], v[204:207], a[72:75], v[128:143]\n"
    "s_add_u32 m0, s48, 0x20000\n"
    "s_add_u32 s49, s63, 0x7e000\n"
    "buffer_load_dwordx4 v224, s[28:31], s49 offen lds\n"
    "v_mfma_f32_32x32x16_bf16 v[144:159], v[204:207], a[76:79], v[144:159]\n"
    "s_waitcnt vmcnt(14)\n"
    "s_barrier\n"
    "s_cmp_lg_u32 s44, 0\n"
    "s_cbranch_scc1 .Lk1\n"
    "s_lshl_b32 s50, s46, 2\n"
    "s_add_u32 s50, s50, s12\n"
    "s_mov_b64 exec, 1\n"
    "v_mov_b32 v242, s52\n"
    "buffer_store_dword v242, off, s[32:35], s50 offset:0 sc1\n"
    "s_mov_b64 exec, -1\n"
    ".Lk1:\n"
    "ds_read_b64_tr_b16 v[192:193], v229 offset:49152\n"
    "ds_read_b64_tr_b16 v[194:195], v229 offset:50176\n"
    "s_waitcnt lgkmcnt(6)\n"
    "v_mfma_f32_32x32x16_bf16 v[96:111], v[176:179], a[80:83], v[96:111]\n"
    "buffer_load_dwordx4 v225, s[28:31], s49 offen offset:1024 lds\n"
    "v_mfma_f32_32x32x16_bf16 v[112:127], v[176:179], a[84:87], v[112:127]\n"
    "s_add_u32 m0, s48, 0x24000\n"
    "s_add_u32 s49, s63, 0x82800\n"
    "buffer_load_dwordx4 v224, s[28:31], s49 offen lds\n"
    "ds_read_b64_tr_b16 v[196:197], v246 offset:49152\n"
    "ds_read_b64_tr_b16 v[198:199], v246 offset:50176\n"
    "v_mfma_f32_32x32x16_bf16 v[128:143], v[180:183], a[80:83], v[128:143]\n"
    "buffer_load_dwordx4 v225, s[28:31], s49 offen offset:1024 lds\n"
    "v_mfma_f32_32x32x16_bf16 v[144:159], v[180:183], a[84:87], v[144:159]\n"
    "s_add_u32 m0, s48, 0x26000\n"
    "s_add_u32 s49, s63, 0x1b000\n"
    "buffer_load_dwordx4 v224, s[28:31], s49 offen lds\n"
    "ds_read_b64_tr_b16 v[200:201], v231 offset:8192\n"
    "ds_read_b64_tr_b16 v[202:203], v231 offset:9216\n"
    "s_waitcnt lgkmcnt(6)\n"
    "v_mfma_f32_32x32x16_bf16 v[96:111], v[184:187], a[88:91], v[96:111]\n"
    "buffer_load_dwordx4 v225, s[28:31], s49 offen offset:1024 lds\n"
    "v_mfma_f32_32x32x16_bf16 v[112:127], v[184:187], a[92:95], v[112:127]\n"
    "s_add_u32 m0, s48, 0x8000\n"
    "s_add_u32 s49, s63, 0x1f800\n"
    "buffer_load_dwordx4 v224, s[28:31], s49 offen lds\n"
    "ds_read_b64_tr_b16 v[204:205], v247 offset:8192\n"
    "ds_read_b64_tr_b16 v[206:207], v247 offset:9216\n"
    "v_mfma_f32_32x32x16_bf16 v[128:143], v[188:191], a[88:91], v[128:143]\n"
    "buffer_load_dwordx4 v225, s[28:31], s49 offen offset:1024 lds\n"
    "v_mfma_f32_32x32x16_bf16 v[144:159], v[188:191], a[92:95], v[144:159]\n"
    "s_add_u32 m0, s48, 0xe000\n"
    "s_add_u32 s49, s63, 0x3f000\n"
    "buffer_load_dwordx4 v224, s[28:31], s49 offen lds\n"
    "ds_read_b64_tr_b16 v[176:177], v228 offset:0\n"
    "ds_read_b64_tr_b16 v[178:179], v228 offset:1024\n"
    "s_waitcnt lgkmcnt(6)\n"
    "v_mfma_f32_32x32x16_bf16 v[96:111], v[192:195], a[96:99], v[96:111]\n"
    "v_mfma_f32_32x32x16_bf16 v[112:127], v[192:195], a[100:103], v[112:127]\n"
    "buffer_load_dwordx4 v225, s[28:31], s49 offen offset:1024 lds\n"
    "ds_read_b64_tr_b16 v[180:181], v232 offset:0\n"
    "ds_read_b64_tr_b16 v[182:183], v232 offset:1024\n"
    "v_mfma_f32_32x32x16_bf16 v[128:143], v[196:199], a[96:99], v[128:143]\n"
    "s_add_u32 m0, s48, 0x14000\n"
    "s_add_u32 s49, s63, 0x43800\n"
    "buffer_load_dwordx4 v224, s[28:31], s49 offen lds\n"
    "v_mfma_f32_32x32x16_bf16 v[144:159], v[196:199], a[100:103], v[144:159]\n"
    "ds_read_b64_tr_b16 v[184:185], v229 offset:0\n"
    "ds_read_b64_tr_b16 v[186:187], v229 offset:1024\n"
    "s_waitcnt lgkmcnt(6)\n"
    "v_mfma_f32_32x32x16_bf16 v[96:111], v[200:203], a[104:107], v[96:111]\n"
    "buffer_load_dwordx4 v225, s[28:31], s49 offen offset:1024 lds\n"
    "v_mfma_f32_32x32x16_bf16 v[112:127], v[200:203], a[108:111], v[112:127]\n"
    "ds_read_b64_tr_b16 v[188:189], v246 offset:0\n"
    "ds_read_b64_tr_b16 v[190:191], v246 offset:1024\n"
    "v_mfma_f32_32x32x16_bf16 v[128:143], v[204:207], a[104:107], v[128:143]\n"
    "s_add_u32 m0, s48, 0x1c000\n"
    "s_add_u32 s49, s63, 0x63000\n"
    "buffer_load_dwordx4 v224, s[28:31], s49 offen lds\n"
    "v_mfma_f32_32x32x16_bf16 v[144:159], v[204:207], a[108:111], v[144:159]\n"
    "ds_read_b64_tr_b16 v[192:193], v229 offset:24576\n"
    "ds_read_b64_tr_b16 v[194:195], v229 offset:25600\n"
    "s_waitcnt lgkmcnt(6)\n"
    "v_mfma_f32_32x32x16_bf16 v[96:111], v[176:179], a[112:115], v[96:111]\n"
    "buffer_load_dwordx4 v225, s[28:31], s49 offen offset:1024 lds\n"
    "v_mfma_f32_32x32x16_bf16 v[112:127], v[176:179], a[116:119], v[112:127]\n"
    "ds_read_b64_tr_b16 v[196:197], v246 offset:24576\n"
    "ds_read_b64_tr_b16 v[198:199], v246 offset:25600\n"
    "v_mfma_f32_32x32x16_bf16 v[128:143], v[180:183], a[112:115], v[128:143]\n"
    "s_add_u32 m0, s48, 0x22000\n"
    "s_add_u32 s49, s63, 0x67800\n"
    "buffer_load_dwordx4 v224, s[28:31], s49 offen lds\n"
    "v_mfma_f32_32x32x16_bf16 v[144:159], v[180:183], a[116:119], v[144:159]\n"
    "ds_read_b64_tr_b16 v[200:201], v228 offset:8192\n"
    "ds_read_b64_tr_b16 v[202:203], v228 offset:9216\n"
    "s_waitcnt lgkmcnt(6)\n"
    "v_mfma_f32_32x32x16_bf16 v[96:111], v[184:187], a[120:123], v[96:111]\n"
    "buffer_load_dwordx4 v225, s[28:31], s49 offen offset:1024 lds\n"
    "v_mfma_f32_32x32x16_bf16 v[112:127], v[184:187], a[124:127], v[112:127]\n"
    "ds_read_b64_tr_b16 v[204:205], v232 offset:8192\n"
    "ds_read_b64_tr_b16 v[206:207], v232 offset:9216\n"
    "v_mfma_f32_32x32x16_bf16 v[128:143], v[188:191], a[120:123], v[128:143]\n"
    "s_add_u32 m0, s48, 0\n"
    "s_add_u32 s49, s63, 0x87000\n"
    "buffer_load_dwordx4 v224, s[28:31], s49 offen lds\n"
    "v_mfma_f32_32x32x16_bf16 v[144:159], v[188:191], a[124:127], v[144:159]\n"
    "ds_read_b64_tr_b16 v[176:177], v228 offset:40960\n"
    "ds_read_b64_tr_b16 v[178:179], v228 offset:41984\n"
    "s_waitcnt lgkmcnt(6)\n"
    "v_mfma_f32_32x32x16_bf16 v[96:111], v[192:195], a[128:131], v[96:111]\n"
    "buffer_load_dwordx4 v225, s[28:31], s49 offen offset:1024 lds\n"
    "s_cmp_lg_u32 s44, 1\n"
    "s_cbranch_scc1 .Lk2\n"
    "buffer_load_dword v243, v240, s[32:35], s12 offen sc1\n"
    ".Lk2:\n"
    "v_mfma_f32_32x32x16_bf16 v[112:127], v[192:195], a[132:135], v[112:127]\n"
    "ds_read_b64_tr_b16 v[180:181], v232 offset:40960\n"
    "ds_read_b64_tr_b16 v[182:183], v232 offset:41984\n"
    "v_mfma_f32_32x32x16_bf16 v[128:143], v[196:199], a[128:131], v[128:143]\n"
    "s_add_u32 m0, s48, 0x10000\n"
    "s_add_u32 s49, s63, 0x8b800\n"
    "buffer_load_dwordx4 v224, s[28:31], s49 offen lds\n"
    "v_mfma_f32_32x32x16_bf16 v[144:159], v[196:199], a[132:135], v[144:159]\n"
    "s_waitcnt vmcnt(31) lgkmcnt(4)\n"
    "ds_read_b64_tr_b16 v[184:185], v228 offset:49152\n"
    "ds_read_b64_tr_b16 v[186:187], v228 offset:50176\n"
    "v_mfma_f32_32x32x16_bf16 v[96:111], v[200:203], a[136:139], v[96:111]\n"
    "buffer_load_dwordx4 v225, s[28:31], s49 offen offset:1024 lds\n"
    "v_mfma_f32_32x32x16_bf16 v[112:127], v[200:203], a[140:143], v[112:127]\n"
    "ds_read_b64_tr_b16 v[188:189], v232 offset:49152\n"
    "ds_read_b64_tr_b16 v[190:191], v232 offset:50176\n"
    "v_mfma_f32_32x32x16_bf16 v[128:143], v[204:207], a[136:139], v[128:143]\n"
    "s_add_u32 m0, s48, 0x16000\n"
    "s_lshl_b32 s49, s46, 7\n"
    "v_add_u32 v224, s49, v241\n"
    "v_add_u32 v225, 0x2000, v224\n"
    "s_add_u32 s49, s64, 0\n"
    "buffer_load_dwordx4 v224, s[28:31], s49 offen lds\n"
    "v_mfma_f32_32x32x16_bf16 v[144:159], v[204:207], a[140:143], v[144:159]\n"
    "s_waitcnt vmcnt(31) lgkmcnt(4)\n"
    "ds_read_b64_tr_b16 v[192:193], v229 offset:8192\n"
    "ds_read_b64_tr_b16 v[194:195], v229 offset:9216\n"
    "v_mfma_f32_32x32x16_bf16 v[96:111], v[176:179], a[144:147], v[96:111]\n"
    "buffer_load_dwordx4 v225, s[28:31], s49 offen offset:1024 lds\n"
    "v_mfma_f32_32x32x16_bf16 v[112:127], v[176:179], a[148:151], v[112:127]\n"
    "ds_read_b64_tr_b16 v[196:197], v246 offset:8192\n"
    "ds_read_b64_tr_b16 v[198:199], v246 offset:9216\n"
    "v_mfma_f32_32x32x16_bf16 v[128:143], v[180:183], a[144:147], v[128:143]\n"
    "s_add_u32 m0, s48, 0x2000\n"
    "s_add_u32 s49, s64, 0x48000\n"
    "buffer_load_dwordx4 v224, s[28:31], s49 offen lds\n"
    "v_mfma_f32_32x32x16_bf16 v[144:159], v[180:183], a[148:151], v[144:159]\n"
    "s_waitcnt vmcnt(31) lgkmcnt(4)\n"
    "ds_read_b64_tr_b16 v[200:201], v229 offset:57344\n"
    "ds_read_b64_tr_b16 v[202:203], v229 offset:58368\n"
    "v_mfma_f32_32x32x16_bf16 v[96:111], v[184:187], a[152:155], v[96:111]\n"
    "buffer_load_dwordx4 v225, s[28:31], s49 offen offset:1024 lds\n"
    "v_mfma_f32_32x32x16_bf16 v[112:127], v[184:187], a[156:159], v[112:127]\n"
    "ds_read_b64_tr_b16 v[204:205], v246 offset:57344\n"
    "ds_read_b64_tr_b16 v[206:207], v246 offset:58368\n"
    "v_mfma_f32_32x32x16_bf16 v[128:143], v[188:191], a[152:155], v[128:143]\n"
    "s_add_u32 m0, s48, 0xa000\n"
    "s_add_u32 s49, s64, 0x9000\n"
    "buffer_load_dwordx4 v224, s[28:31], s49 offen lds\n"
    "v_mfma_f32_32x32x16_bf16 v[144:159], v[188:191], a[156:159], v[144:159]\n"
    "s_waitcnt vmcnt(31) lgkmcnt(4)\n"
    "ds_read_b64_tr_b16 v[176:177], v228 offset:16384\n"
    "ds_read_b64_tr_b16 v[178:179], v228 offset:17408\n"
    "v_mfma_f32_32x32x16_bf16 v[96:111], v[192:195], a[160:163], v[96:111]\n"
    "buffer_load_dwordx4 v225, s[28:31], s49 offen offset:1024 lds\n"
    "v_mfma_f32_32x32x16_bf16 v[112:127], v[192:195], a[164:167], v[112:127]\n"
    "ds_read_b64_tr_b16 v[180:181], v232 offset:16384\n"
    "ds_read_b64_tr_b16 v[182:183], v232 offset:17408\n"
    "v_mfma_f32_32x32x16_bf16 v[128:143], v[196:199], a[160:163], v[128:143]\n"
    "s_add_u32 m0, s48, 0xc000\n"
    "s_add_u32 s49, s64, 0x51000\n"
    "buffer_load_dwordx4 v224, s[28:31], s49 offen lds\n"
    "v_mfma_f32_32x32x16_bf16 v[144:159], v[196:199], a[164:167], v[144:159]\n"
    "s_waitcnt vmcnt(31) lgkmcnt(4)\n"
    "ds_read_b64_tr_b16 v[184:185], v228 offset:24576\n"
    "ds_read_b64_tr_b16 v[186:187], v228 offset:25600\n"
    "v_mfma_f32_32x32x16_bf16 v[96:111], v[200:203], a[168:171], v[96:111]\n"
    "buffer_load_dwordx4 v225, s[28:31], s49 offen offset:1024 lds\n"
    "s_cmp_lg_u32 s44, 1\n"
    "s_cbranch_scc1 .Lk4\n"
    "s_waitcnt vmcnt(10)\n"
    "s_movk_i32 s50, 0x4e20\n"
    ".Lk3:\n"
    "v_cmp_ne_u32 s[66:67], s52, v243\n"
    "s_nop 0\n"
    "s_and_b64 s[66:67], s[66:67], 0xff\n"
    "s_cbranch_scc0 .Lk4\n"
    "buffer_load_dword v243, v240, s[32:35], s12 offen sc1\n"
    "s_sub_u32 s50, s50, 1\n"
    "s_waitcnt vmcnt(0)\n"
    "s_cmp_lg_u32 s50, 0\n"
    "s_cbranch_scc1 .Lk3\n"
    ".Lk4:\n"
    "s_barrier\n"
    "s_cmp_lg_u32 s44, 3\n"
    "s_cbranch_scc1 .Lk5\n"
    "buffer_load_dwordx4 v[64:67], v239, s[32:35], s14 offen offset:0 sc1\n"
    "buffer_load_dwordx4 v[68:71], v239, s[32:35], s14 offen offset:1024 sc1\n"
    "buffer_load_dwordx4 v[72:75], v239, s[32:35], s14 offen offset:2048 sc1\n"
    "buffer_load_dwordx4 v[76:79], v239, s[32:35], s14 offen offset:3072 sc1\n"
    "s_add_u32 s14, s14, 0x1000\n"
    "buffer_load_dwordx4 v[80:83], v239, s[32:35], s14 offen offset:0 sc1\n"
    "buffer_load_dwordx4 v[84:87], v239, s[32:35], s14 offen offset:1024 sc1\n"
    "buffer_load_dwordx4 v[88:91], v239, s[32:35], s14 offen offset:2048 sc1\n"
    "buffer_load_dwordx4 v[92:95], v239, s[32:35], s14 offen offset:3072 sc1\n"
    ".Lk5:\n"
    "v_mfma_f32_32x32x16_bf16 v[112:127], v[200:203], a[172:175], v[112:127]\n"
    "s_add_u32 s50, s11, 0\n"
    "buffer_load_dwordx4 v[32:35], v239, s[32:35], s50 offen offset:0 sc1\n"
    "buffer_load_dwordx4 v[36:39], v239, s[32:35], s50 offen offset:1024 sc1\n"
    "ds_read_b64_tr_b16 v[188:189], v232 offset:24576\n"
    "ds_read_b64_tr_b16 v[190:191], v232 offset:25600\n"
    "v_mfma_f32_32x32x16_bf16 v[128:143], v[204:207], a[168:171], v[128:143]\n"
    "s_add_u32 m0, s48, 0x12000\n"
    "s_add_u32 s49, s65, 0x90000\n"
    "buffer_load_dwordx4 v224, s[28:31], s49 offen lds\n"
    "v_mfma_f32_32x32x16_bf16 v[144:159], v[204:207], a[172:175], v[144:159]\n"
    "s_add_u32 s50, s11, 0x2000\n"
    "buffer_load_dwordx4 v[40:43], v239, s[32:35], s50 offen offset:0 sc1\n"
    "buffer_load_dwordx4 v[44:47], v239, s[32:35], s50 offen offset:1024 sc1\n"
    "s_waitcnt vmcnt(35) lgkmcnt(4)\n"
    "ds_read_b64_tr_b16 v[192:193], v229 offset:32768\n"
    "ds_read_b64_tr_b16 v[194:195], v229 offset:33792\n"
    "v_mfma_f32_32x32x16_bf16 v[96:111], v[176:179], a[176:179], v[96:111]\n"
    "buffer_load_dwordx4 v225, s[28:31], s49 offen offset:1024 lds\n"
    "v_mfma_f32_32x32x16_bf16 v[112:127], v[176:179], a[180:183], v[112:127]\n"
    "s_add_u32 s50, s11, 0x4000\n"
    "buffer_load_dwordx4 v[48:51], v239, s[32:35], s50 offen offset:0 sc1\n"
    "buffer_load_dwordx4 v[52:55], v239, s[32:35], s50 offen offset:1024 sc1\n"
    "ds_read_b64_tr_b16 v[196:197], v246 offset:32768\n"
    "ds_read_b64_tr_b16 v[198:199], v246 offset:33792\n"
    "v_mfma_f32_32x32x16_bf16 v[128:143], v[180:183], a[176:179], v[128:143]\n"
    "s_add_u32 m0, s48, 0x1e000\n"
    "s_add_u32 s49, s64, 0x12000\n"
    "buffer_load_dwordx4 v224, s[28:31], s49 offen lds\n"
    "v_mfma_f32_32x32x16_bf16 v[144:159], v[180:183], a[180:183], v[144:159]\n"
    "s_waitcnt vmcnt(37) lgkmcnt(4)\n"
    "ds_read_b64_tr_b16 v[200:201], v229 offset:40960\n"
    "ds_read_b64_tr_b16 v[202:203], v229 offset:41984\n"
    "v_mfma_f32_32x32x16_bf16 v[96:111], v[184:187], a[184:187], v[96:111]\n"
    "buffer_load_dwordx4 v225, s[28:31], s49 offen offset:1024 lds\n"
    "v_mfma_f32_32x32x16_bf16 v[112:127], v[184:187], a[188:191], v[112:127]\n"
    "ds_read_b64_tr_b16 v[204:205], v246 offset:40960\n"
    "ds_read_b64_tr_b16 v[206:207], v246 offset:41984\n"
    "v_mfma_f32_32x32x16_bf16 v[128:143], v[188:191], a[184:187], v[128:143]\n"
    "s_add_u32 m0, s48, 0x4000\n"
    "s_add_u32 s49, s64, 0x5a000\n"
    "buffer_load_dwordx4 v224, s[28:31], s49 offen lds\n"
    "v_mfma_f32_32x32x16_bf16 v[144:159], v[188:191], a[188:191], v[144:159]\n"
    "s_waitcnt vmcnt(37) lgkmcnt(4)\n"
    "ds_read_b64_tr_b16 v[176:177], v231 offset:0\n"
    "ds_read_b64_tr_b16 v[178:179], v231 offset:1024\n"
    "v_mfma_f32_32x32x16_bf16 v[96:111], v[192:195], a[192:195], v[96:111]\n"
    "buffer_load_dwordx4 v225, s[28:31], s49 offen offset:1024 lds\n"
    "v_mfma_f32_32x32x16_bf16 v[112:127], v[192:195], a[196:199], v[112:127]\n"
    "ds_read_b64_tr_b16 v[180:181], v247 offset:0\n"
    "ds_read_b64_tr_b16 v[182:183], v247 offset:1024\n"
    "v_mfma_f32_32x32x16_bf16 v[128:143], v[196:199], a[192:195], v[128:143]\n"
    "s_add_u32 m0, s48, 0x6000\n"
    "s_add_u32 s49, s64, 0x1b000\n"
    "buffer_load_dwordx4 v224, s[28:31], s49 offen lds\n"
    "v_mfma_f32_32x32x16_bf16 v[144:159], v[196:199], a[196:199], v[144:159]\n"
    "s_waitcnt vmcnt(37) lgkmcnt(4)\n"
    "ds_read_b64_tr_b16 v[184:185], v231 offset:16384\n"
    "ds_read_b64_tr_b16 v[186:187], v231 offset:17408\n"
    "v_mfma_f32_32x32x16_bf16 v[96:111], v[200:203], a[200:203], v[96:111]\n"
    "buffer_load_dwordx4 v225, s[28:31], s49 offen offset:1024 lds\n"
    "v_mfma_f32_32x32x16_bf16 v[112:127], v[200:203], a[204:207], v[112:127]\n"
    "ds_read_b64_tr_b16 v[188:189], v247 offset:16384\n"
    "ds_read_b64_tr_b16 v[190:191], v247 offset:17408\n"
    "v_mfma_f32_32x32x16_bf16 v[128:143], v[204:207], a[200:203], v[128:143]\n"
    "s_add_u32 m0, s48, 0x18000\n"
    "s_add_u32 s49, s64, 0x63000\n"
    "buffer_load_dwordx4 v224, s[28:31], s49 offen lds\n"
    "v_mfma_f32_32x32x16_bf16 v[144:159], v[204:207], a[204:207], v[144:159]\n"
    "s_waitcnt vmcnt(37) lgkmcnt(4)\n"
    "ds_read_b64_tr_b16 v[192:193], v231 offset:24576\n"
    "ds_read_b64_tr_b16 v[194:195], v231 offset:25600\n"
    "v_mfma_f32_32x32x16_bf16 v[96:111], v[176:179], a[208:211], v[96:111]\n"
    "buffer_load_dwordx4 v225, s[28:31], s49 offen offset:1024 lds\n"
    "v_mfma_f32_32x32x16_bf16 v[112:127], v[176:179], a[212:215], v[112:127]\n"
    "ds_read_b64_tr_b16 v[196:197], v247 offset:24576\n"
    "ds_read_b64_tr_b16 v[198:199], v247 offset:25600\n"
    "v_mfma_f32_32x32x16_bf16 v[128:143], v[180:183], a[208:211], v[128:143]\n"
    "v_mfma_f32_32x32x16_bf16 v[144:159], v[180:183], a[212:215], v[144:159]\n"
    "s_waitcnt vmcnt(36) lgkmcnt(4)\n"
    "ds_read_b64_tr_b16 v[200:201], v228 offset:32768\n"
    "ds_read_b64_tr_b16 v[202:203], v228 offset:33792\n"
    "v_mfma_f32_32x32x16_bf16 v[96:111], v[184:187], a[216:219], v[96:111]\n"
    "v_mfma_f32_32x32x16_bf16 v[112:127], v[184:187], a[220:223], v[112:127]\n"
    "ds_read_b64_tr_b16 v[204:205], v232 offset:32768\n"
    "ds_read_b64_tr_b16 v[206:207], v232 offset:33792\n"
    "v_mfma_f32_32x32x16_bf16 v[128:143], v[188:191], a[216:219], v[128:143]\n"
    "v_mfma_f32_32x32x16_bf16 v[144:159], v[188:191], a[220:223], v[144:159]\n"
    "s_waitcnt vmcnt(34) lgkmcnt(4)\n"
    "ds_read_b64_tr_b16 v[176:177], v228 offset:57344\n"
    "ds_read_b64_tr_b16 v[178:179], v228 offset:58368\n"
    "v_mfma_f32_32x32x16_bf16 v[96:111], v[192:195], a[224:227], v[96:111]\n"
    "v_mfma_f32_32x32x16_bf16 v[112:127], v[192:195], a[228:231], v[112:127]\n"
    "ds_read_b64_tr_b16 v[180:181], v232 offset:57344\n"
    "ds_read_b64_tr_b16 v[182:183], v232 offset:58368\n"
    "v_mfma_f32_32x32x16_bf16 v[128:143], v[196:199], a[224:227], v[128:143]\n"
    "v_mfma_f32_32x32x16_bf16 v[144:159], v[196:199], a[228:231], v[144:159]\n"
    "s_waitcnt vmcnt(32) lgkmcnt(4)\n"
    "ds_read_b64_tr_b16 v[184:185], v229 offset:16384\n"
    "ds_read_b64_tr_b16 v[186:187], v229 offset:17408\n"
    "v_mfma_f32_32x32x16_bf16 v[96:111], v[200:203], a[232:235], v[96:111]\n"
    "v_mfma_f32_32x32x16_bf16 v[112:127], v[200:203], a[236:239], v[112:127]\n"
    "ds_read_b64_tr_b16 v[188:189], v246 offset:16384\n"
    "ds_read_b64_tr_b16 v[190:191], v246 offset:17408\n"
    "v_mfma_f32_32x32x16_bf16 v[128:143], v[204:207], a[232:235], v[128:143]\n"
    "v_mfma_f32_32x32x16_bf16 v[144:159], v[204:207], a[236:239], v[144:159]\n"
    "s_waitcnt vmcnt(30) lgkmcnt(4)\n"
    "ds_read_b64_tr_b16 v[192:193], v229 offset:49152\n"
    "ds_read_b64_tr_b16 v[194:195], v229 offset:50176\n"
    "v_mfma_f32_32x32x16_bf16 v[96:111], v[176:179], a[240:243], v[96:111]\n"
    "v_mfma_f32_32x32x16_bf16 v[112:127], v[176:179], a[244:247], v[112:127]\n"
    "ds_read_b64_tr_b16 v[196:197], v246 offset:49152\n"
    "ds_read_b64_tr_b16 v[198:199], v246 offset:50176\n"
    "v_mfma_f32_32x32x16_bf16 v[128:143], v[180:183], a[240:243], v[128:143]\n"
    "v_mfma_f32_32x32x16_bf16 v[144:159], v[180:183], a[244:247], v[144:159]\n"
    "s_waitcnt vmcnt(28) lgkmcnt(4)\n"
    "ds_read_b64_tr_b16 v[200:201], v231 offset:8192\n"
    "ds_read_b64_tr_b16 v[202:203], v231 offset:9216\n"
    "v_mfma_f32_32x32x16_bf16 v[96:111], v[184:187], a[248:251], v[96:111]\n"
    "v_mfma_f32_32x32x16_bf16 v[112:127], v[184:187], a[252:255], v[112:127]\n"
    "ds_read_b64_tr_b16 v[204:205], v247 offset:8192\n"
    "ds_read_b64_tr_b16 v[206:207], v247 offset:9216\n"
    "v_mfma_f32_32x32x16_bf16 v[128:143], v[188:191], a[248:251], v[128:143]\n"
    "v_mfma_f32_32x32x16_bf16 v[144:159], v[188:191], a[252:255], v[144:159]\n"
    "s_waitcnt vmcnt(26) lgkmcnt(4)\n"
    "ds_read_b64_tr_b16 v[176:177], v228 offset:0\n"
    "ds_read_b64_tr_b16 v[178:179], v228 offset:1024\n"
    "v_mfma_f32_32x32x16_bf16 v[96:111], v[192:195], v[0:3], v[96:111]\n"
    "v_mfma_f32_32x32x16_bf16 v[112:127], v[192:195], v[4:7], v[112:127]\n"
    "ds_read_b64_tr_b16 v[180:181], v232 offset:0\n"
    "ds_read_b64_tr_b16 v[182:183], v232 offset:1024\n"
    "v_mfma_f32_32x32x16_bf16 v[128:143], v[196:199], v[0:3], v[128:143]\n"
    "v_mfma_f32_32x32x16_bf16 v[144:159], v[196:199], v[4:7], v[144:159]\n"
    "s_waitcnt vmcnt(24) lgkmcnt(4)\n"
    "ds_read_b64_tr_b16 v[184:185], v229 offset:0\n"
    "ds_read_b64_tr_b16 v[186:187], v229 offset:1024\n"
    "v_mfma_f32_32x32x16_bf16 v[96:111], v[200:203], v[8:11], v[96:111]\n"
    "v_mfma_f32_32x32x16_bf16 v[112:127], v[200:203], v[12:15], v[112:127]\n"
    "ds_read_b64_tr_b16 v[188:189], v246 offset:0\n"
    "ds_read_b64_tr_b16 v[190:191], v246 offset:1024\n"
    "v_mfma_f32_32x32x16_bf16 v[128:143], v[204:207], v[8:11], v[128:143]\n"
    "v_mfma_f32_32x32x16_bf16 v[144:159], v[204:207], v[12:15], v[144:159]\n"
    "s_waitcnt vmcnt(20)\n"
    "s_barrier\n"
    "v_add_u32 v233, 0x16000, v233\n"
    "ds_read_b64_tr_b16 v[192:193], v233 offset:0\n"
    "ds_read_b64_tr_b16 v[194:195], v233 offset:1024\n"
    "s_waitcnt lgkmcnt(6)\n"
    "v_mfma_f32_32x32x16_bf16 v[96:111], v[176:179], v[16:19], v[96:111]\n"
    "ds_read_b64_tr_b16 v[196:197], v233 offset:2048\n"
    "ds_read_b64_tr_b16 v[198:199], v233 offset:3072\n"
    "v_mfma_f32_32x32x16_bf16 v[112:127], v[176:179], v[20:23], v[112:127]\n"
    "ds_read_b64_tr_b16 v[200:201], v233 offset:4096\n"
    "ds_read_b64_tr_b16 v[202:203], v233 offset:5120\n"
    "v_mfma_f32_32x32x16_bf16 v[128:143], v[180:183], v[16:19], v[128:143]\n"
    "ds_read_b64_tr_b16 v[204:205], v233 offset:6144\n"
    "ds_read_b64_tr_b16 v[206:207], v233 offset:7168\n"
    "v_mfma_f32_32x32x16_bf16 v[144:159], v[180:183], v[20:23], v[144:159]\n"
    "v_add_u32 v233, 0xfffec000, v233\n"
    "ds_read_b64_tr_b16 v[176:177], v233 offset:0\n"
    "ds_read_b64_tr_b16 v[178:179], v233 offset:1024\n"
    "s_waitcnt lgkmcnt(10)\n"
    "v_mfma_f32_32x32x16_bf16 v[96:111], v[184:187], v[24:27], v[96:111]\n"
    "ds_read_b64_tr_b16 v[180:181], v233 offset:2048\n"
    "ds_read_b64_tr_b16 v[182:183], v233 offset:3072\n"
    "v_mfma_f32_32x32x16_bf16 v[112:127], v[184:187], v[28:31], v[112:127]\n"
    "ds_read_b64_tr_b16 v[184:185], v233 offset:4096\n"
    "ds_read_b64_tr_b16 v[186:187], v233 offset:5120\n"
    "v_mfma_f32_32x32x16_bf16 v[128:143], v[188:191], v[24:27], v[128:143]\n"
    "v_mfma_f32_32x32x16_bf16 v[144:159], v[188:191], v[28:31], v[144:159]\n"
    "ds_read_b64_tr_b16 v[188:189], v233 offset:6144\n"
    "ds_read_b64_tr_b16 v[190:191], v233 offset:7168\n"
    "s_waitcnt lgkmcnt(12)\n"
    "v_mfma_f32_32x32x16_bf16 v[160:175], v[192:195], a[0:3], 0\n"
    "s_waitcnt vmcnt(16)\n"
    "s_barrier\n"
    "ds_read_b64_tr_b16 v[192:193], v233 offset:32768\n"
    "ds_read_b64_tr_b16 v[194:195], v233 offset:33792\n"
    "v_mfma_f32_32x32x16_bf16 v[160:175], v[196:199], a[8:11], v[160:175]\n"
    "s_add_u32 s50, s46, s44\n"
    "s_add_u32 s50, s50, 4\n"
    "s_and_b32 s50, s50, 7\n"
    "s_lshl_b32 s8, s45, 3\n"
    "s_add_u32 s50, s50, s8\n"
    "s_mul_i32 s50, s50, 7\n"
    "s_add_u32 s50, s50, s44\n"
    "s_add_u32 s50, s50, 3\n"
    "s_lshl_b32 s50, s50, 13\n"
    "s_movk_i32 s10, 0x400\n"
    "s_add_u32 s50, s50, s68\n"
    "s_lshl_b32 s9, s10, 1\n"
    "s_mul_i32 s8, s9, s47\n"
    "s_sub_u32 s9, s9, s8\n"
    "s_add_u32 s8, s8, s50\n"
    "s_add_u32 s9, s9, s50\n"
    "v_cvt_pk_bf16_f32 v96, v96, v97\n"
    "v_cvt_pk_bf16_f32 v97, v98, v99\n"
    "v_cvt_pk_bf16_f32 v98, v100, v101\n"
    "v_cvt_pk_bf16_f32 v99, v102, v103\n"
    "buffer_store_dwordx4 v[96:99], v239, s[32:35], s8 offen sc1\n"
    "ds_read_b64_tr_b16 v[196:197], v233 offset:34816\n"
    "ds_read_b64_tr_b16 v[198:199], v233 offset:35840\n"
    "s_waitcnt lgkmcnt(12)\n"
    "v_mfma_f32_32x32x16_bf16 v[160:175], v[200:203], a[16:19], v[160:175]\n"
    "v_cvt_pk_bf16_f32 v100, v104, v105\n"
    "v_cvt_pk_bf16_f32 v101, v106, v107\n"
    "v_cvt_pk_bf16_f32 v102, v108, v109\n"
    "v_cvt_pk_bf16_f32 v103, v110, v111\n"
    "s_mul_i32 s50, s10, 1\n"
    "s_add_u32 s50, s50, s8\n"
    "buffer_store_dwordx4 v[100:103], v239, s[32:35], s50 offen sc1\n"
    "ds_read_b64_tr_b16 v[200:201], v233 offset:36864\n"
    "ds_read_b64_tr_b16 v[202:203], v233 offset:37888\n"
    "v_mfma_f32_32x32x16_bf16 v[160:175], v[204:207], a[24:27], v[160:175]\n"
    "v_cvt_pk_bf16_f32 v112, v112, v113\n"
    "v_cvt_pk_bf16_f32 v113, v114, v115\n"
    "v_cvt_pk_bf16_f32 v114, v116, v117\n"
    "v_cvt_pk_bf16_f32 v115, v118, v119\n"
    "buffer_store_dwordx4 v[112:115], v239, s[32:35], s9 offen sc1\n"
    "ds_read_b64_tr_b16 v[204:205], v233 offset:38912\n"
    "ds_read_b64_tr_b16 v[206:207], v233 offset:39936\n"
    "s_waitcnt lgkmcnt(12)\n"
    "v_mfma_f32_32x32x16_bf16 v[160:175], v[176:179], a[32:35], v[160:175]\n"
    "v_cvt_pk_bf16_f32 v116, v120, v121\n"
    "v_cvt_pk_bf16_f32 v117, v122, v123\n"
    "v_cvt_pk_bf16_f32 v118, v124, v125\n"
    "v_cvt_pk_bf16_f32 v119, v126, v127\n"
    "s_mul_i32 s50, s10, 1\n"
    "s_add_u32 s50, s50, s9\n"
    "buffer_store_dwordx4 v[116:119], v239, s[32:35], s50 offen sc1\n"
    "ds_read_b64_tr_b16 v[176:177], v233 offset:40960\n"
    "ds_read_b64_tr_b16 v[178:179], v233 offset:41984\n"
    "v_mfma_f32_32x32x16_bf16 v[160:175], v[180:183], a[40:43], v[160:175]\n"
    "v_cvt_pk_bf16_f32 v128, v128, v129\n"
    "v_cvt_pk_bf16_f32 v129, v130, v131\n"
    "v_cvt_pk_bf16_f32 v130, v132, v133\n"
    "v_cvt_pk_bf16_f32 v131, v134, v135\n"
    "s_mul_i32 s50, s10, 4\n"
    "s_add_u32 s50, s50, s8\n"
    "buffer_store_dwordx4 v[128:131], v239, s[32:35], s50 offen sc1\n"
    "ds_read_b64_tr_b16 v[180:181], v233 offset:43008\n"
    "ds_read_b64_tr_b16 v[182:183], v233 offset:44032\n"
    "s_waitcnt lgkmcnt(12)\n"
    "v_mfma_f32_32x32x16_bf16 v[160:175], v[184:187], a[48:51], v[160:175]\n"
    "v_cvt_pk_bf16_f32 v132, v136, v137\n"
    "v_cvt_pk_bf16_f32 v133, v138, v139\n"
    "v_cvt_pk_bf16_f32 v134, v140, v141\n"
    "v_cvt_pk_bf16_f32 v135, v142, v143\n"
    "s_mul_i32 s50, s10, 5\n"
    "s_add_u32 s50, s50, s8\n"
    "buffer_store_dwordx4 v[132:135], v239, s[32:35], s50 offen sc1\n"
    "ds_read_b64_tr_b16 v[184:185], v233 offset:45056\n"
    "ds_read_b64_tr_b16 v[186:187], v233 offset:46080\n"
    "v_mfma_f32_32x32x16_bf16 v[160:175], v[188:191], a[56:59], v[160:175]\n"
    "v_cvt_pk_bf16_f32 v144, v144, v145\n"
    "v_cvt_pk_bf16_f32 v145, v146, v147\n"
    "v_cvt_pk_bf16_f32 v146, v148, v149\n"
    "v_cvt_pk_bf16_f32 v147, v150, v151\n"
    "s_mul_i32 s50, s10, 4\n"
    "s_add_u32 s50, s50, s9\n"
    "buffer_store_dwordx4 v[144:147], v239, s[32:35], s50 offen sc1\n"
    "ds_read_b64_tr_b16 v[188:189], v233 offset:47104\n"
    "ds_read_b64_tr_b16 v[190:191], v233 offset:48128\n"
    "s_waitcnt lgkmcnt(12)\n"
    "v_mfma_f32_32x32x16_bf16 v[160:175], v[192:195], a[64:67], v[160:175]\n"
    "v_cvt_pk_bf16_f32 v148, v152, v153\n"
    "v_cvt_pk_bf16_f32 v149, v154, v155\n"
    "v_cvt_pk_bf16_f32 v150, v156, v157\n"
    "v_cvt_pk_bf16_f32 v151, v158, v159\n"
    "s_mul_i32 s50, s10, 5\n"
    "s_add_u32 s50, s50, s9\n"
    "buffer_store_dwordx4 v[148:151], v239, s[32:35], s50 offen sc1\n"
    "s_waitcnt vmcnt(14)\n"
    "s_barrier\n"
    "v_add_u32 v233, 0x10000, v233\n"
    "ds_read_b64_tr_b16 v[192:193], v233 offset:0\n"
    "ds_read_b64_tr_b16 v[194:195], v233 offset:1024\n"
    "v_mfma_f32_32x32x16_bf16 v[160:175], v[196:199], a[72:75], v[160:175]\n"
    "ds_read_b64_tr_b16 v[196:197], v233 offset:2048\n"
    "ds_read_b64_tr_b16 v[198:199], v233 offset:3072\n"
    "s_waitcnt lgkmcnt(12)\n"
    "v_mfma_f32_32x32x16_bf16 v[160:175], v[200:203], a[80:83], v[160:175]\n"
    "ds_read_b64_tr_b16 v[200:201], v233 offset:4096\n"
    "ds_read_b64_tr_b16 v[202:203], v233 offset:5120\n"
    "v_mfma_f32_32x32x16_bf16 v[160:175], v[204:207], a[88:91], v[160:175]\n"
    "ds_read_b64_tr_b16 v[204:205], v233 offset:6144\n"
    "ds_read_b64_tr_b16 v[206:207], v233 offset:7168\n"
    "s_waitcnt lgkmcnt(12)\n"
    "v_mfma_f32_32x32x16_bf16 v[160:175], v[176:179], a[96:99], v[160:175]\n"
    "ds_read_b64_tr_b16 v[176:177], v233 offset:49152\n"
    "ds_read_b64_tr_b16 v[178:179], v233 offset:50176\n"
    "v_mfma_f32_32x32x16_bf16 v[160:175], v[180:183], a[104:107], v[160:175]\n"
    "ds_read_b64_tr_b16 v[180:181], v233 offset:51200\n"
    "ds_read_b64_tr_b16 v[182:183], v233 offset:52224\n"
    "s_waitcnt lgkmcnt(12)\n"
    "v_mfma_f32_32x32x16_bf16 v[160:175], v[184:187], a[112:115], v[160:175]\n"
    "ds_read_b64_tr_b16 v[184:185], v233 offset:53248\n"
    "ds_read_b64_tr_b16 v[186:187], v233 offset:54272\n"
    "v_mfma_f32_32x32x16_bf16 v[160:175], v[188:191], a[120:123], v[160:175]\n"
    "ds_read_b64_tr_b16 v[188:189], v233 offset:55296\n"
    "ds_read_b64_tr_b16 v[190:191], v233 offset:56320\n"
    "s_waitcnt lgkmcnt(12)\n"
    "v_mfma_f32_32x32x16_bf16 v[160:175], v[192:195], a[128:131], v[160:175]\n"
    "s_waitcnt vmcnt(10)\n"
    "s_barrier\n"
    "v_add_u32 v233, 0xffff2000, v233\n"
    "ds_read_b64_tr_b16 v[192:193], v233 offset:0\n"
    "ds_read_b64_tr_b16 v[194:195], v233 offset:1024\n"
    "v_mfma_f32_32x32x16_bf16 v[160:175], v[196:199], a[136:139], v[160:175]\n"
    "ds_read_b64_tr_b16 v[196:197], v233 offset:2048\n"
    "ds_read_b64_tr_b16 v[198:199], v233 offset:3072\n"
    "s_waitcnt lgkmcnt(12)\n"
    "v_mfma_f32_32x32x16_bf16 v[160:175], v[200:203], a[144:147], v[160:175]\n"
    "ds_read_b64_tr_b16 v[200:201], v233 offset:4096\n"
    "ds_read_b64_tr_b16 v[202:203], v233 offset:5120\n"
    "v_mfma_f32_32x32x16_bf16 v[160:175], v[204:207], a[152:155], v[160:175]\n"
    "ds_read_b64_tr_b16 v[204:205], v233 offset:6144\n"
    "ds_read_b64_tr_b16 v[206:207], v233 offset:7168\n"
    "s_waitcnt lgkmcnt(12)\n"
    "v_mfma_f32_32x32x16_bf16 v[160:175], v[176:179], a[160:163], v[160:175]\n"
    "ds_read_b64_tr_b16 v[176:177], v233 offset:8192\n"
    "ds_read_b64_tr_b16 v[178:179], v233 offset:9216\n"
    "v_mfma_f32_32x32x16_bf16 v[160:175], v[180:183], a[168:171], v[160:175]\n"
    "ds_read_b64_tr_b16 v[180:181], v233 offset:10240\n"
    "ds_read_b64_tr_b16 v[182:183], v233 offset:11264\n"
    "s_waitcnt lgkmcnt(12)\n"
    "v_mfma_f32_32x32x16_bf16 v[160:175], v[184:187], a[176:179], v[160:175]\n"
    "ds_read_b64_tr_b16 v[184:185], v233 offset:12288\n"
    "ds_read_b64_tr_b16 v[186:187], v233 offset:13312\n"
    "v_mfma_f32_32x32x16_bf16 v[160:175], v[188:191], a[184:187], v[160:175]\n"
    "ds_read_b64_tr_b16 v[188:189], v233 offset:14336\n"
    "ds_read_b64_tr_b16 v[190:191], v233 offset:15360\n"
    "s_waitcnt lgkmcnt(12)\n"
    "v_mfma_f32_32x32x16_bf16 v[160:175], v[192:195], a[192:195], v[160:175]\n"
    "s_waitcnt vmcnt(8)\n"
    "s_barrier\n"
    "v_add_u32 v233, 0x14000, v233\n"
    "ds_read_b64_tr_b16 v[192:193], v233 offset:0\n"
    "ds_read_b64_tr_b16 v[194:195], v233 offset:1024\n"
    "v_mfma_f32_32x32x16_bf16 v[160:175], v[196:199], a[200:203], v[160:175]\n"
    "ds_read_b64_tr_b16 v[196:197], v233 offset:2048\n"
    "ds_read_b64_tr_b16 v[198:199], v233 offset:3072\n"
    "s_waitcnt lgkmcnt(12)\n"
    "v_mfma_f32_32x32x16_bf16 v[160:175], v[200:203], a[208:211], v[160:175]\n"
    "ds_read_b64_tr_b16 v[200:201], v233 offset:4096\n"
    "ds_read_b64_tr_b16 v[202:203], v233 offset:5120\n"
    "v_mfma_f32_32x32x16_bf16 v[160:175], v[204:207], a[216:219], v[160:175]\n"
    "ds_read_b64_tr_b16 v[204:205], v233 offset:6144\n"
    "ds_read_b64_tr_b16 v[206:207], v233 offset:7168\n"
    "s_waitcnt lgkmcnt(12)\n"
    "v_mfma_f32_32x32x16_bf16 v[160:175], v[176:179], a[224:227], v[160:175]\n"
    "v_mfma_f32_32x32x16_bf16 v[160:175], v[180:183], a[232:235], v[160:175]\n"
    "s_waitcnt lgkmcnt(8)\n"
    "v_mfma_f32_32x32x16_bf16 v[160:175], v[184:187], a[240:243], v[160:175]\n"
    "v_mfma_f32_32x32x16_bf16 v[160:175], v[188:191], a[248:251], v[160:175]\n"
    "s_waitcnt lgkmcnt(4)\n"
    "v_mfma_f32_32x32x16_bf16 v[160:175], v[192:195], v[0:3], v[160:175]\n"
    "v_mfma_f32_32x32x16_bf16 v[160:175], v[196:199], v[8:11], v[160:175]\n"
    "s_waitcnt lgkmcnt(0)\n"
    "v_mfma_f32_32x32x16_bf16 v[160:175], v[200:203], v[16:19], v[160:175]\n"
    "v_mfma_f32_32x32x16_bf16 v[160:175], v[204:207], v[24:27], v[160:175]\n"
    "s_waitcnt vmcnt(0)\n"
    "s_barrier\n"
    "s_cmp_lg_u32 s44, 0\n"
    "s_cbranch_scc1 .Lk6\n"
    "s_lshl_b32 s50, s46, 2\n"
    "s_add_u32 s50, s50, s12\n"
    "s_mov_b64 exec, 1\n"
    "v_mov_b32 v242, s52\n"
    "buffer_store_dword v242, off, s[32:35], s50 offset:32 sc1\n"
    "s_mov_b64 exec, -1\n"
    ".Lk6:\n"
    "s_cmp_lg_u32 s44, 3\n"
    "s_cbranch_scc1 .Lk7\n"
    "v_mfma_f32_32x32x16_bf16 v[144:159], v[216:219], v[64:67], 0\n"
    "v_mfma_f32_32x32x16_bf16 v[144:159], v[216:219], v[68:71], v[144:159]\n"
    "v_mfma_f32_32x32x16_bf16 v[144:159], v[216:219], v[72:75], v[144:159]\n"
    "v_mfma_f32_32x32x16_bf16 v[144:159], v[216:219], v[76:79], v[144:159]\n"
    "v_mfma_f32_32x32x16_bf16 v[144:159], v[216:219], v[80:83], v[144:159]\n"
    "v_mfma_f32_32x32x16_bf16 v[144:159], v[216:219], v[84:87], v[144:159]\n"
    "v_mfma_f32_32x32x16_bf16 v[144:159], v[216:219], v[88:91], v[144:159]\n"
    "v_mfma_f32_32x32x16_bf16 v[144:159], v[216:219], v[92:95], v[144:159]\n"
    "s_nop 11\n"
    "v_cvt_pk_bf16_f32 v144, v144, v145\n"
    "v_cvt_pk_bf16_f32 v145, v146, v147\n"
    "v_cvt_pk_bf16_f32 v146, v148, v149\n"
    "v_cvt_pk_bf16_f32 v147, v150, v151\n"
    "buffer_store_dwordx2 v[144:145], v245, s[36:39], s15 offen offset:0\n"
    "buffer_store_dwordx2 v[146:147], v245, s[36:39], s15 offen offset:16\n"
    ".Lk7:\n"
    "s_cmp_lg_u32 s44, 1\n"
    "s_cbranch_scc1 .Lk8\n"
    "buffer_load_dword v243, v240, s[32:35], s12 offen sc1\n"
    ".Lk8:\n"
    "v_mfma_f32_32x32x16_bf16 v[160:175], v[216:219], v[32:35], v[160:175]\n"
    "v_mfma_f32_32x32x16_bf16 v[160:175], v[220:223], v[36:39], v[160:175]\n"
    "v_mfma_f32_32x32x16_bf16 v[160:175], v[216:219], v[40:43], v[160:175]\n"
    "v_mfma_f32_32x32x16_bf16 v[160:175], v[220:223], v[44:47], v[160:175]\n"
    "v_mfma_f32_32x32x16_bf16 v[160:175], v[216:219], v[48:51], v[160:175]\n"
    "v_mfma_f32_32x32x16_bf16 v[160:175], v[220:223], v[52:55], v[160:175]\n"
    "s_cmp_lg_u32 s44, 1\n"
    "s_cbranch_scc1 .Lk10\n"
    "s_movk_i32 s50, 0x4e20\n"
    ".Lk11:\n"
    "s_waitcnt vmcnt(0)\n"
    "v_cmp_ne_u32 s[66:67], s52, v243\n"
    "s_nop 0\n"
    "s_and_b64 s[66:67], s[66:67], s[18:19]\n"
    "s_cbranch_scc0 .Lk12\n"
    "buffer_load_dword v243, v240, s[32:35], s12 offen sc1\n"
    "s_sub_u32 s50, s50, 1\n"
    "s_cmp_lg_u32 s50, 0\n"
    "s_cbranch_scc1 .Lk11\n"
    ".Lk12:\n"
    ".Lk10:\n"
    "s_barrier\n"
    "s_add_u32 s50, s11, 0x6000\n"
    "buffer_load_dwordx4 v[32:35], v239, s[32:35], s50 offen offset:0 sc1\n"
    "buffer_load_dwordx4 v[36:39], v239, s[32:35], s50 offen offset:1024 sc1\n"
    "s_add_u32 s50, s11, 0x8000\n"
    "buffer_load_dwordx4 v[40:43], v239, s[32:35], s50 offen offset:0 sc1\n"
    "buffer_load_dwordx4 v[44:47], v239, s[32:35], s50 offen offset:1024 sc1\n"
    "s_add_u32 s50, s11, 0xa000\n"
    "buffer_load_dwordx4 v[48:51], v239, s[32:35], s50 offen offset:0 sc1\n"
    "buffer_load_dwordx4 v[52:55], v239, s[32:35], s50 offen offset:1024 sc1\n"
    "s_add_u32 s50, s11, 0xc000\n"
    "buffer_load_dwordx4 v[56:59], v239, s[32:35], s50 offen offset:0 sc1\n"
    "buffer_load_dwordx4 v[60:63], v239, s[32:35], s50 offen offset:1024 sc1\n"
    "s_lshl_b32 s50, s46, 2\n"
    "s_add_u32 s50, s50, s44\n"
    "s_cmp_lg_u32 s50, 0\n"
    "s_cbranch_scc1 .Lk13\n"
    "s_lshl_b32 s50, s45, 7\n"
    "s_mov_b64 exec, 1\n"
    "v_mov_b32 v242, s52\n"
    "buffer_store_dword v242, off, s[32:35], s50 sc1\n"
    "s_mov_b64 exec, -1\n"
    ".Lk13:\n"
    "s_waitcnt vmcnt(7)\n"
    "v_mfma_f32_32x32x16_bf16 v[160:175], v[216:219], v[32:35], v[160:175]\n"
    "s_waitcnt vmcnt(6)\n"
    "v_mfma_f32_32x32x16_bf16 v[160:175], v[220:223], v[36:39], v[160:175]\n"
    "s_waitcnt vmcnt(5)\n"
    "v_mfma_f32_32x32x16_bf16 v[160:175], v[216:219], v[40:43], v[160:175]\n"
    "s_waitcnt vmcnt(4)\n"
    "v_mfma_f32_32x32x16_bf16 v[160:175], v[220:223], v[44:47], v[160:175]\n"
    "s_waitcnt vmcnt(3)\n"
    "v_mfma_f32_32x32x16_bf16 v[160:175], v[216:219], v[48:51], v[160:175]\n"
    "s_waitcnt vmcnt(2)\n"
    "v_mfma_f32_32x32x16_bf16 v[160:175], v[220:223], v[52:55], v[160:175]\n"
    "s_waitcnt vmcnt(1)\n"
    "v_mfma_f32_32x32x16_bf16 v[160:175], v[216:219], v[56:59], v[160:175]\n"
    "s_waitcnt vmcnt(0)\n"
    "v_mfma_f32_32x32x16_bf16 v[160:175], v[220:223], v[60:63], v[160:175]\n"
    "s_nop 11\n"
    "v_cvt_pk_bf16_f32 v160, v160, v161\n"
    "v_cvt_pk_bf16_f32 v161, v162, v163\n"
    "v_cvt_pk_bf16_f32 v162, v164, v165\n"
    "v_cvt_pk_bf16_f32 v163, v166, v167\n"
    "v_cvt_pk_bf16_f32 v164, v168, v169\n"
    "v_cvt_pk_bf16_f32 v165, v170, v171\n"
    "v_cvt_pk_bf16_f32 v166, v172, v173\n"
    "v_cvt_pk_bf16_f32 v167, v174, v175\n"
    "buffer_store_dwordx2 v[160:161], v245, s[36:39], s13 offen offset:0\n"
    "buffer_store_dwordx2 v[162:163], v245, s[36:39], s13 offen offset:16\n"
    "buffer_store_dwordx2 v[164:165], v245, s[36:39], s13 offen offset:32\n"
    "buffer_store_dwordx2 v[166:167], v245, s[36:39], s13 offen offset:48\n"
    "s_endpgm\n"
    ".Lend:\n"
    :: "s"(__builtin_amdgcn_kernarg_segment_ptr()), "s"(bid), "s"(w)
    : "memory", "vcc", "scc", "m0", "s8", "s9", "s10", "s11", "s12", "s13", "s14", "s15", "s16", "s17", "s18", "s19", "s20", "s21", "s22", "s23", "s24", "s25", "s26", "s27", "s28", "s29", "s30", "s31", "s32", "s33", "s34", "s35", "s36", "s37", "s38", "s39", "s40", "s41", "s42", "s43", "s44", "s45", "s46", "s47", "s48", "s49", "s50", "s51", "s52", "s53", "s54", "s55", "s56", "s57", "s58", "s59", "s60", "s61", "s62", "s63", "s64", "s65", "s66", "s67", "s68", "s69", "s70", "s71", "s72", "s73", "s74", "s75", "s76", "s77", "s78", "s79", "s80", "s81", "s82", "s83", "s84", "s85", "s86", "s87", "s88", "s89", "s90", "s91", "s92", "s93", "s94", "s95", "s96", "s97", "s98", "s99", "s100", "s101", "v0", "v1", "v2", "v3", "v4", "v5", "v6", "v7", "v8", "v9", "v10", "v11", "v12", "v13", "v14", "v15", "v16", "v17", "v18", "v19", "v20", "v21", "v22", "v23", "v24", "v25", "v26", "v27", "v28", "v29", "v30", "v31", "v32", "v33", "v34", "v35", "v36", "v37", "v38", "v39", "v40", "v41", "v42", "v43", "v44", "v45", "v46", "v47", "v48", "v49", "v50", "v51", "v52", "v53", "v54", "v55", "v56", "v57", "v58", "v59", "v60", "v61", "v62", "v63", "v64", "v65", "v66", "v67", "v68", "v69", "v70", "v71", "v72", "v73", "v74", "v75", "v76", "v77", "v78", "v79", "v80", "v81", "v82", "v83", "v84", "v85", "v86", "v87", "v88", "v89", "v90", "v91", "v92", "v93", "v94", "v95", "v96", "v97", "v98", "v99", "v100", "v101", "v102", "v103", "v104", "v105", "v106", "v107", "v108", "v109", "v110", "v111", "v112", "v113", "v114", "v115", "v116", "v117", "v118", "v119", "v120", "v121", "v122", "v123", "v124", "v125", "v126", "v127", "v128", "v129", "v130", "v131", "v132", "v133", "v134", "v135", "v136", "v137", "v138", "v139", "v140", "v141", "v142", "v143", "v144", "v145", "v146", "v147", "v148", "v149", "v150", "v151", "v152", "v153", "v154", "v155", "v156", "v157", "v158", "v159", "v160", "v161", "v162", "v163", "v164", "v165", "v166", "v167", "v168", "v169", "v170", "v171", "v172", "v173", "v174", "v175", "v176", "v177", "v178", "v179", "v180", "v181", "v182", "v183", "v184", "v185", "v186", "v187", "v188", "v189", "v190", "v191", "v192", "v193", "v194", "v195", "v196", "v197", "v198", "v199", "v200", "v201", "v202", "v203", "v204", "v205", "v206", "v207", "v208", "v209", "v210", "v211", "v212", "v213", "v214", "v215", "v216", "v217", "v218", "v219", "v220", "v221", "v222", "v223", "v224", "v225", "v226", "v227", "v228", "v229", "v230", "v231", "v232", "v233", "v234", "v235", "v236", "v237", "v238", "v239", "v240", "v241", "v242", "v243", "v244", "v245", "v246", "v247", "v248", "v249", "v250", "v251", "v252", "v253", "v254", "v255", "a0", "a1", "a2", "a3", "a4", "a5", "a6", "a7", "a8", "a9", "a10", "a11", "a12", "a13", "a14", "a15", "a16", "a17", "a18", "a19", "a20", "a21", "a22", "a23", "a24", "a25", "a26", "a27", "a28", "a29", "a30", "a31", "a32", "a33", "a34", "a35", "a36", "a37", "a38", "a39", "a40", "a41", "a42", "a43", "a44", "a45", "a46", "a47", "a48", "a49", "a50", "a51", "a52", "a53", "a54", "a55", "a56", "a57", "a58", "a59", "a60", "a61", "a62", "a63", "a64", "a65", "a66", "a67", "a68", "a69", "a70", "a71", "a72", "a73", "a74", "a75", "a76", "a77", "a78", "a79", "a80", "a81", "a82", "a83", "a84", "a85", "a86", "a87", "a88", "a89", "a90", "a91", "a92", "a93", "a94", "a95", "a96", "a97", "a98", "a99", "a100", "a101", "a102", "a103", "a104", "a105", "a106", "a107", "a108", "a109", "a110", "a111", "a112", "a113", "a114", "a115", "a116", "a117", "a118", "a119", "a120", "a121", "a122", "a123", "a124", "a125", "a126", "a127", "a128", "a129", "a130", "a131", "a132", "a133", "a134", "a135", "a136", "a137", "a138", "a139", "a140", "a141", "a142", "a143", "a144", "a145", "a146", "a147", "a148", "a149", "a150", "a151", "a152", "a153", "a154", "a155", "a156", "a157", "a158", "a159", "a160", "a161", "a162", "a163", "a164", "a165", "a166", "a167", "a168", "a169", "a170", "a171", "a172", "a173", "a174", "a175", "a176", "a177", "a178", "a179", "a180", "a181", "a182", "a183", "a184", "a185", "a186", "a187", "a188", "a189", "a190", "a191", "a192", "a193", "a194", "a195", "a196", "a197", "a198", "a199", "a200", "a201", "a202", "a203", "a204", "a205", "a206", "a207", "a208", "a209", "a210", "a211", "a212", "a213", "a214", "a215", "a216", "a217", "a218", "a219", "a220", "a221", "a222", "a223", "a224", "a225", "a226", "a227", "a228", "a229", "a230", "a231", "a232", "a233", "a234", "a235", "a236", "a237", "a238", "a239", "a240", "a241", "a242", "a243", "a244", "a245", "a246", "a247", "a248", "a249", "a250", "a251", "a252", "a253", "a254", "a255");
}

extern "C" void kernel_launch(void* const* d_in, const int* in_sizes, int n_in, void* d_out, int out_size, void* d_ws, size_t ws_size, hipStream_t stream) {
  (void)in_sizes; (void)n_in; (void)out_size; (void)ws_size;
  hipFuncSetAttribute((const void*)mlp_fused, hipFuncAttributeMaxDynamicSharedMemorySize, 163840);
  mlp_fused<<<256, 256, 163840, stream>>>(d_in[0], d_in[1], d_in[2], d_out, d_ws);
}
```
